```python
import torch

def _softmax(x, axis=-1):
    return torch.softmax(x, dim=axis)
def _gelu(x):
    return torch.nn.functional.gelu(x, approximate="tanh")
def _silu(x):
    return torch.nn.functional.silu(x)
def _sigmoid(x):
    return torch.sigmoid(x)
def _layernorm(x, axis=-1, eps=1e-5):
    m = x.mean(dim=axis, keepdim=True)
    v = x.var(dim=axis, keepdim=True, unbiased=False)
    return (x - m) / torch.sqrt(v + eps)
def _scatter_add(tgt, idx, src, axis=0):
    return tgt.clone().index_add_(axis, idx, src)
def _segment_sum(src, seg, n):
    out = torch.zeros((n,) + src.shape[1:], dtype=src.dtype)
    return out.index_add_(0, seg, src)
def _pad(x, pw):
    flat = tuple(v for pair in reversed(pw) for v in pair)
    return torch.nn.functional.pad(x, flat)

kernel_name = 'standalone__glu_gated_swiglu__decode'

def setup_inputs():

    _w = (lambda a: (lambda t: t.to(torch.bfloat16) if t.is_floating_point() else t)(torch.as_tensor(a)))
    B, S, D, F = 8, 1, 1024, 3584
    return {
        "x": _w((lambda *s: torch.randn(*s, dtype=torch.bfloat16) * 0.05)(B, S, D)),
        "w1": _w((lambda *s: torch.randn(*s, dtype=torch.bfloat16) * 0.05)(D, F) * (2.0 / D) ** 0.5),
        "w3": _w((lambda *s: torch.randn(*s, dtype=torch.bfloat16) * 0.05)(D, F) * (2.0 / D) ** 0.5),
        "w2": _w((lambda *s: torch.randn(*s, dtype=torch.bfloat16) * 0.05)(F, D) * (2.0 / F) ** 0.5),
    }

def reference(x, w1, w3, w2):
    gate = torch.einsum("bsd,df->bsf", x, w1)
    up = torch.einsum("bsd,df->bsf", x, w3)
    h = _silu(gate) * up
    out = torch.einsum("bsf,fd->bsd", h, w2)
    return out
```

```cpp
#include <hip/hip_runtime.h>
#include <hip/hip_bf16.h>
#include <stdint.h>

constexpr int B_ = 8, D_ = 1024, F_ = 3584;
typedef unsigned short u16;
__device__ __forceinline__ float bf2f(u16 v) { return __uint_as_float(((unsigned)v) << 16); }
__device__ __forceinline__ u16 f2bf(float f) { __hip_bfloat16 b = __float2bfloat16(f); return *reinterpret_cast<u16*>(&b); }

__global__ void __launch_bounds__(256) k1(const u16* __restrict__ x, const u16* __restrict__ w1, const u16* __restrict__ w3, u16* __restrict__ h) {
    __shared__ float xs[B_][D_];
    __shared__ float red[2][B_][16][17];
    int t = threadIdx.x;
    for (int i = t; i < B_ * D_; i += 256) xs[i / D_][i % D_] = bf2f(x[i]);
    __syncthreads();
    int bid = blockIdx.x;
    int f0 = bid * 16;
    int c = t & 15, rg = t >> 4;
    float g[B_], u[B_];
    for (int b = 0; b < B_; b++) g[b] = u[b] = 0.f;
    for (int k = rg * 64; k < rg * 64 + 64; k++) {
        float a = bf2f(w1[(size_t)k * F_ + f0 + c]);
        float e = bf2f(w3[(size_t)k * F_ + f0 + c]);
        for (int b = 0; b < B_; b++) { g[b] += xs[b][k] * a; u[b] += xs[b][k] * e; }
    }
    for (int b = 0; b < B_; b++) { red[0][b][c][rg] = g[b]; red[1][b][c][rg] = u[b]; }
    __syncthreads();
    if (t < 128) {
        int b = t >> 4, cc = t & 15;
        float gs = 0, us = 0;
        for (int r = 0; r < 16; r++) { gs += red[0][b][cc][r]; us += red[1][b][cc][r]; }
        gs = bf2f(f2bf(gs)); us = bf2f(f2bf(us));
        float s = gs / (1.f + __expf(-gs));
        s = bf2f(f2bf(s));
        h[b * F_ + f0 + cc] = f2bf(s * us);
    }
}
__global__ void __launch_bounds__(256) k2(const u16* __restrict__ h, const u16* __restrict__ w2, u16* __restrict__ out) {
    __shared__ float red[B_][16][17];
    int t = threadIdx.x;
    int d0 = blockIdx.x * 16;
    int c = t & 15, rg = t >> 4;
    float acc[B_];
    for (int b = 0; b < B_; b++) acc[b] = 0.f;
    for (int k = rg * 224; k < rg * 224 + 224; k++) {
        float a = bf2f(w2[(size_t)k * D_ + d0 + c]);
        for (int b = 0; b < B_; b++) acc[b] += bf2f(h[b * F_ + k]) * a;
    }
    for (int b = 0; b < B_; b++) red[b][c][rg] = acc[b];
    __syncthreads();
    if (t < 128) {
        int b = t >> 4, cc = t & 15;
        float s = 0;
        for (int r = 0; r < 16; r++) s += red[b][cc][r];
        out[b * D_ + d0 + cc] = f2bf(s);
    }
}

extern "C" void kernel_launch(void* const* d_in, const int* in_sizes, int n_in, void* d_out, int out_size, void* d_ws, size_t ws_size, hipStream_t stream) {
    const u16* x = (const u16*)d_in[0];
    const u16* w1 = (const u16*)d_in[1];
    const u16* w3 = (const u16*)d_in[2];
    const u16* w2 = (const u16*)d_in[3];
    u16* h = (u16*)d_ws;
    k1<<<224, 256, 0, stream>>>(x, w1, w3, h);
    k2<<<64, 256, 0, stream>>>(h, w2, (u16*)d_out);
}
```

```cpp
#include <hip/hip_runtime.h>
#include <stdint.h>

#define GAS __attribute__((address_space(1)))
#define LAS __attribute__((address_space(3)))
typedef float f32x4 __attribute__((ext_vector_type(4)));
typedef float f32x2 __attribute__((ext_vector_type(2)));
typedef short s16x4 __attribute__((ext_vector_type(4)));
typedef short s16x8 __attribute__((ext_vector_type(8)));
typedef __bf16 bf16x8 __attribute__((ext_vector_type(8)));
typedef __bf16 bf16x2 __attribute__((ext_vector_type(2)));
typedef unsigned u32x4 __attribute__((ext_vector_type(4)));
typedef unsigned u32x2 __attribute__((ext_vector_type(2)));
typedef unsigned long long u64;
typedef GAS u64 gu64;
typedef GAS unsigned gu32;
#define RLX_AGENT __ATOMIC_RELAXED, __HIP_MEMORY_SCOPE_AGENT

constexpr int D_ = 1024, F_ = 3584;
constexpr int NWG = 224, G_ = 14, M_ = 16;
constexpr int L_X = 0, L_W13 = 16384, L_W2 = 81920, L_TOTAL = 114688;
constexpr int WS_EPOCH = 0, WS_H = 4096, WS_P = WS_H + NWG * 512;
constexpr unsigned SPIN_CAP = 1u << 18;

#define W13_AUX ""
#define W2_AUX " nt"

#define STAMP_AT(i) do {} while (0)

__device__ __forceinline__ u32x4 make_srd(const void* p, unsigned bytes) {
    const u64 a = (u64)p;
    u32x4 r;
    r.x = (unsigned)a; r.y = (unsigned)(a >> 32) & 0xffffu; r.z = bytes; r.w = 0x00020000u;
    return r;
}
#define DMA16(AUX, ldsoff, voff, srd, soff) \
    asm volatile("s_mov_b32 m0, %0\n\ts_nop 0\n\tbuffer_load_dwordx4 %1, %2, %3 offen" AUX " lds" \
                 :: "s"(ldsoff), "v"(voff), "s"(srd), "s"(soff) : "memory")
#define WAIT_VM(n) asm volatile("s_waitcnt vmcnt(" #n ")" ::: "memory")

__device__ __forceinline__ s16x4 tr_read(unsigned addr) {
    typedef short v4i16_t __attribute__((ext_vector_type(4)));
    return __builtin_amdgcn_ds_read_tr16_b64_v4i16((LAS v4i16_t*)addr);
}
__device__ __forceinline__ bf16x8 cat(s16x4 a, s16x4 b) {
    s16x8 r = __builtin_shufflevector(a, b, 0, 1, 2, 3, 4, 5, 6, 7);
    return __builtin_bit_cast(bf16x8, r);
}
__device__ __forceinline__ float bf_round(float f) { return (float)(__bf16)f; }

template <int S> struct P1 {
    static __device__ __forceinline__ void run(unsigned wa, unsigned xa0, unsigned xa1, f32x4& ag, f32x4& au) {
        if constexpr (S == 0) WAIT_VM(22); else if constexpr (S == 1) WAIT_VM(20); else if constexpr (S == 2) WAIT_VM(18);
        else if constexpr (S == 3) WAIT_VM(16); else if constexpr (S == 4) WAIT_VM(14); else if constexpr (S == 5) WAIT_VM(12);
        else if constexpr (S == 6) WAIT_VM(10); else WAIT_VM(8);
        const bf16x8 xb = *(const LAS bf16x8*)(((S & 1) ? xa1 : xa0) + (S >> 1) * 1024);
        const bf16x8 wg = cat(tr_read(wa + S * 2048), tr_read(wa + S * 2048 + 512));
        const bf16x8 wu = cat(tr_read(wa + S * 2048 + 1024), tr_read(wa + S * 2048 + 1536));
        ag = __builtin_amdgcn_mfma_f32_16x16x32_bf16(wg, xb, ag, 0, 0, 0);
        au = __builtin_amdgcn_mfma_f32_16x16x32_bf16(wu, xb, au, 0, 0, 0);
        if constexpr (S < 7) P1<S + 1>::run(wa, xa0, xa1, ag, au);
    }
};

__global__ void __launch_bounds__(256, 1)
swiglu_fused(const void* __restrict__ x, const void* __restrict__ w1, const void* __restrict__ w3, const void* __restrict__ w2,
             void* __restrict__ out, unsigned char* __restrict__ ws
) {
    extern __shared__ __attribute__((aligned(16))) unsigned char lds[];
    const unsigned tid = threadIdx.x, lane = tid & 63;
    const unsigned w = __builtin_amdgcn_readfirstlane(tid >> 6);
    const unsigned bid = blockIdx.x;
    const unsigned id = (bid & 7) * 28 + (bid >> 3);
    const unsigned g = id >> 4, m = id & 15;
    STAMP_AT(0);
    const unsigned lbase = (unsigned)(size_t)(LAS unsigned char*)lds;

    unsigned epoch;
    asm volatile("global_load_dword %0, %1, %2 sc1" : "=v"(epoch) : "v"(0u), "s"(ws) : "memory");

    const u32x4 sx = make_srd(x, 8 * D_ * 2), s1 = make_srd(w1, D_ * F_ * 2), s3 = make_srd(w3, D_ * F_ * 2), s2 = make_srd(w2, D_ * F_ * 2);
    {
        const unsigned b = lane >> 3, c = lane & 7;
        const unsigned vx = b * 2048 + ((c ^ ((b >> 1) << 1)) << 4);
#pragma unroll
        for (int i = 0; i < 4; ++i) DMA16("", lbase + L_X + w * 4096 + i * 1024, vx, sx, w * 512 + i * 128);
    }
    {
        const unsigned p = lane >> 1;
        const unsigned row = 8 * ((p >> 2) & 3) + 4 * (p >> 4) + (p & 3);
        const unsigned vw = row * (F_ * 2) + (lane & 1) * 16 + id * 32;
#pragma unroll
        for (int s = 0; s < 8; ++s) {
            const unsigned so = (w * 256 + s * 32) * (F_ * 2);
            DMA16(W13_AUX, lbase + L_W13 + w * 16384 + s * 2048, vw, s1, so);
            DMA16(W13_AUX, lbase + L_W13 + w * 16384 + s * 2048 + 1024, vw, s3, so);
        }
    }
    {
        const unsigned r = lane >> 3, c = lane & 7;
        const unsigned v2 = r * 2048 + ((c ^ ((r >> 1) << 1)) << 4) + m * 128;
#pragma unroll
        for (int i = 0; i < 8; ++i) DMA16(W2_AUX, lbase + L_W2 + w * 8192 + i * 1024, v2, s2, (g * 256 + w * 64 + i * 8) * 2048);
    }
    STAMP_AT(1);

    f32x4 ag = {0.f, 0.f, 0.f, 0.f}, au = {0.f, 0.f, 0.f, 0.f};
    {
        const unsigned wa = lbase + L_W13 + w * 16384 + lane * 8;
        const unsigned b = lane & 7, oct = lane >> 4;
        const unsigned xa0 = lbase + L_X + w * 4096 + b * 128 + ((oct ^ ((b >> 1) << 1)) << 4);
        P1<0>::run(wa, xa0, xa0 ^ 64, ag, au);
    }
    asm volatile("" : "+v"(epoch));
    const unsigned tag = __builtin_amdgcn_readfirstlane(epoch) * 0x9E3779B9u + 0x7F4A7C15u;
    STAMP_AT(2);
    *(LAS f32x4*)(lbase + L_W13 + w * 16384 + lane * 16) = ag;
    *(LAS f32x4*)(lbase + L_W13 + w * 16384 + 1024 + lane * 16) = au;
    asm volatile("s_waitcnt lgkmcnt(0)\n\ts_barrier" ::: "memory");
    if (w == 0) {
        const unsigned pair = lane >> 5, e = (lane >> 4) & 1, sub = (lane >> 3) & 1, b = lane & 7;
        const unsigned ra = lbase + L_W13 + ((2 * sub + e) * 16 + b) * 16 + pair * 8;
        f32x2 gs = *(const LAS f32x2*)(ra), us = *(const LAS f32x2*)(ra + 1024);
#pragma unroll
        for (int i = 1; i < 4; ++i) { gs += *(const LAS f32x2*)(ra + i * 16384); us += *(const LAS f32x2*)(ra + i * 16384 + 1024); }
        bf16x2 hv;
#pragma unroll
        for (int j = 0; j < 2; ++j) {
            const float gv = bf_round(gs[j]), uv = bf_round(us[j]);
            const float sv = bf_round(gv * __builtin_amdgcn_rcpf(1.f + __expf(-gv)));
            hv[j] = (__bf16)(sv * uv);
        }
        gu64* hp = (gu64*)(ws + WS_H + (id >> 1) * 1024 + pair * 512 + (id & 1) * 256 + (lane & 31) * 8);
        __hip_atomic_store(hp, ((u64)tag << 32) | __builtin_bit_cast(unsigned, hv), RLX_AGENT);
    }
    STAMP_AT(3);

    f32x4 acc[4];
    {
        gu64* hp = (gu64*)(ws + WS_H + (g * 8 + w * 2) * 1024 + lane * 8);
        u64 v[4];
#pragma unroll
        for (int i = 0; i < 4; ++i) v[i] = __hip_atomic_load(hp + i * 64, RLX_AGENT);
        WAIT_VM(4);
        STAMP_AT(4);
        bf16x8 wf[2][4];
        {
            const unsigned hh = lane >> 5, e = (lane >> 4) & 1, q = (lane >> 2) & 3, p = lane & 3;
            const unsigned r = 4 * e + q;
            const unsigned a0 = lbase + L_W2 + w * 8192 + hh * 2048 + r * 128 + p * 8;
#pragma unroll
            for (int cb = 0; cb < 4; ++cb) {
                const unsigned a = a0 + ((cb ^ (r >> 1)) << 5);
#pragma unroll
                for (int ks = 0; ks < 2; ++ks) wf[ks][cb] = cat(tr_read(a + ks * 4096), tr_read(a + ks * 4096 + 1024));
            }
        }
        for (unsigned spins = 0;;) {
            bool ok = true;
#pragma unroll
            for (int i = 0; i < 4; ++i) ok &= (unsigned)(v[i] >> 32) == tag;
            if (__all(ok) || ++spins > SPIN_CAP) break;
#pragma unroll
            for (int i = 0; i < 4; ++i) v[i] = __hip_atomic_load(hp + i * 64, RLX_AGENT);
        }
        STAMP_AT(5);
#pragma unroll
        for (int cb = 0; cb < 4; ++cb) acc[cb] = f32x4{0.f, 0.f, 0.f, 0.f};
#pragma unroll
        for (int ks = 0; ks < 2; ++ks) {
            u32x4 hb;
            hb.x = (unsigned)v[2 * ks]; hb.y = (unsigned)v[2 * ks + 1];
            hb.z = __builtin_amdgcn_update_dpp(0u, hb.x, 0x128, 0xf, 0xf, true);
            hb.w = __builtin_amdgcn_update_dpp(0u, hb.y, 0x128, 0xf, 0xf, true);
            const bf16x8 hf = __builtin_bit_cast(bf16x8, hb);
#pragma unroll
            for (int cb = 0; cb < 4; ++cb) acc[cb] = __builtin_amdgcn_mfma_f32_16x16x32_bf16(wf[ks][cb], hf, acc[cb], 0, 0, 0);
        }
    }
#pragma unroll
    for (int cb = 0; cb < 4; ++cb) *(LAS f32x4*)(lbase + L_W13 + w * 16384 + 4096 + cb * 1024 + lane * 16) = acc[cb];
    asm volatile("s_waitcnt lgkmcnt(0)\n\ts_barrier" ::: "memory");
    {
        const unsigned b = lane >> 3, dp = lane & 7;
        const unsigned ra = lbase + L_W13 + 4096 + w * 1024 + ((dp >> 1) * 16 + b) * 16 + (dp & 1) * 8;
        f32x2 s = *(const LAS f32x2*)(ra);
#pragma unroll
        for (int i = 1; i < 4; ++i) s += *(const LAS f32x2*)(ra + i * 16384);
        gu64* pp = (gu64*)(ws + WS_P + ((m * 8 + b) * G_ + g) * 512 + w * 128 + dp * 8);
        const float sx0 = s.x, sy0 = s.y;
        __hip_atomic_store(pp, ((u64)tag << 32) | __float_as_uint(sx0), RLX_AGENT);
        __hip_atomic_store(pp + 8, ((u64)tag << 32) | __float_as_uint(sy0), RLX_AGENT);
    }
    STAMP_AT(6);

    if (g < 8) {
        const unsigned gq = lane >> 3, dp = lane & 7;
        const unsigned g2 = gq < 6 ? gq + 8 : gq;
        gu64* p0 = (gu64*)(ws + WS_P + ((m * 8 + g) * G_ + gq) * 512 + w * 128 + dp * 8);
        gu64* p1 = (gu64*)(ws + WS_P + ((m * 8 + g) * G_ + g2) * 512 + w * 128 + dp * 8);
        u64 v[4];
        for (unsigned spins = 0;;) {
            v[0] = __hip_atomic_load(p0, RLX_AGENT); v[1] = __hip_atomic_load(p0 + 8, RLX_AGENT);
            v[2] = __hip_atomic_load(p1, RLX_AGENT); v[3] = __hip_atomic_load(p1 + 8, RLX_AGENT);
            bool ok = true;
#pragma unroll
            for (int i = 0; i < 4; ++i) ok &= (unsigned)(v[i] >> 32) == tag;
            if (__all(ok) || ++spins > SPIN_CAP) break;
        }
        STAMP_AT(7);
        float s0 = __builtin_bit_cast(float, (unsigned)v[0]), s1v = __builtin_bit_cast(float, (unsigned)v[1]);
        if (gq < 6) { s0 += __builtin_bit_cast(float, (unsigned)v[2]); s1v += __builtin_bit_cast(float, (unsigned)v[3]); }
#pragma unroll
        for (int o = 8; o < 64; o <<= 1) { s0 += __shfl_xor(s0, o); s1v += __shfl_xor(s1v, o); }
        if (lane < 8) {
            bf16x2 o2; o2[0] = (__bf16)s0; o2[1] = (__bf16)s1v;
            *(GAS unsigned*)((GAS unsigned char*)out + (g * D_ + m * 64 + w * 16 + dp * 2) * 2) = __builtin_bit_cast(unsigned, o2);
        }
        if (id == 0 && tid == 0) __hip_atomic_store((gu32*)(ws + WS_EPOCH), tag, RLX_AGENT);
    }
    STAMP_AT(8);
}

extern "C" void kernel_launch(void* const* d_in, const int* in_sizes, int n_in, void* d_out, int out_size, void* d_ws, size_t ws_size,
                              hipStream_t stream) {
    (void)hipFuncSetAttribute((const void*)swiglu_fused, hipFuncAttributeMaxDynamicSharedMemorySize, L_TOTAL);
    swiglu_fused<<<NWG, 256, L_TOTAL, stream>>>(d_in[0], d_in[1], d_in[2], d_in[3], d_out, (unsigned char*)d_ws);
}
```

```cpp
#define DIAG 0
#include <hip/hip_runtime.h>
#include <stdint.h>

#define GAS __attribute__((address_space(1)))
#define LAS __attribute__((address_space(3)))
typedef float f32x4 __attribute__((ext_vector_type(4)));
typedef float f32x2 __attribute__((ext_vector_type(2)));
typedef short s16x4 __attribute__((ext_vector_type(4)));
typedef short s16x8 __attribute__((ext_vector_type(8)));
typedef __bf16 bf16x8 __attribute__((ext_vector_type(8)));
typedef __bf16 bf16x2 __attribute__((ext_vector_type(2)));
typedef unsigned u32x4 __attribute__((ext_vector_type(4)));
typedef unsigned u32x2 __attribute__((ext_vector_type(2)));
typedef unsigned long long u64;
typedef GAS u64 gu64;
typedef GAS unsigned gu32;
#define RLX_AGENT __ATOMIC_RELAXED, __HIP_MEMORY_SCOPE_AGENT

constexpr int D_ = 1024, F_ = 3584;
constexpr int NWG = 224, G_ = 14, M_ = 16;
constexpr int L_X = 0, L_W13 = 16384, L_W2 = 81920, L_TOTAL = 114688;
constexpr int WS_EPOCH = 0, WS_H = 4096, WS_P = WS_H + NWG * 512;
constexpr unsigned SPIN_CAP = 1u << 18;

#define W13_AUX ""
#define W2_AUX ""

#define STAMP_AT(i) asm volatile("s_memrealtime %0\n\ts_waitcnt lgkmcnt(0)" : "=s"(st_[i]) :: "memory")

__device__ __forceinline__ u32x4 make_srd(const void* p, unsigned bytes) {
    const u64 a = (u64)p;
    u32x4 r;
    r.x = (unsigned)a; r.y = (unsigned)(a >> 32) & 0xffffu; r.z = bytes; r.w = 0x00020000u;
    return r;
}
#define DMA16(AUX, ldsoff, voff, srd, soff) \
    asm volatile("s_mov_b32 m0, %0\n\ts_nop 0\n\tbuffer_load_dwordx4 %1, %2, %3 offen" AUX " lds" \
                 :: "s"(ldsoff), "v"(voff), "s"(srd), "s"(soff) : "memory")
#define WAIT_VM(n) asm volatile("s_waitcnt vmcnt(" #n ")" ::: "memory")

__device__ __forceinline__ s16x4 tr_read(unsigned addr) {
    typedef short v4i16_t __attribute__((ext_vector_type(4)));
    return __builtin_amdgcn_ds_read_tr16_b64_v4i16((LAS v4i16_t*)addr);
}
__device__ __forceinline__ bf16x8 cat(s16x4 a, s16x4 b) {
    s16x8 r = __builtin_shufflevector(a, b, 0, 1, 2, 3, 4, 5, 6, 7);
    return __builtin_bit_cast(bf16x8, r);
}
__device__ __forceinline__ float bf_round(float f) { return (float)(__bf16)f; }

template <int S> struct P1 {
    static __device__ __forceinline__ void run(unsigned wa, unsigned xa0, unsigned xa1, f32x4& ag, f32x4& au) {
        if constexpr (S == 0) WAIT_VM(22); else if constexpr (S == 1) WAIT_VM(20); else if constexpr (S == 2) WAIT_VM(18);
        else if constexpr (S == 3) WAIT_VM(16); else if constexpr (S == 4) WAIT_VM(14); else if constexpr (S == 5) WAIT_VM(12);
        else if constexpr (S == 6) WAIT_VM(10); else WAIT_VM(8);
        const bf16x8 xb = *(const LAS bf16x8*)(((S & 1) ? xa1 : xa0) + (S >> 1) * 1024);
        const bf16x8 wg = cat(tr_read(wa + S * 2048), tr_read(wa + S * 2048 + 512));
        const bf16x8 wu = cat(tr_read(wa + S * 2048 + 1024), tr_read(wa + S * 2048 + 1536));
        ag = __builtin_amdgcn_mfma_f32_16x16x32_bf16(wg, xb, ag, 0, 0, 0);
        au = __builtin_amdgcn_mfma_f32_16x16x32_bf16(wu, xb, au, 0, 0, 0);
        if constexpr (S < 7) P1<S + 1>::run(wa, xa0, xa1, ag, au);
    }
};

__global__ void __launch_bounds__(256, 1)
swiglu_fused(const void* __restrict__ x, const void* __restrict__ w1, const void* __restrict__ w3, const void* __restrict__ w2,
             void* __restrict__ out, unsigned char* __restrict__ ws
) {
    extern __shared__ __attribute__((aligned(16))) unsigned char lds[];
    const unsigned tid = threadIdx.x, lane = tid & 63;
    const unsigned w = __builtin_amdgcn_readfirstlane(tid >> 6);
    const unsigned bid = blockIdx.x;
    const unsigned id = (bid & 7) * 28 + (bid >> 3);
    const unsigned g = id >> 4, m = id & 15;
    u64 st_[10] = {0, 0, 0, 0, 0, 0, 0, 0, 0, 0};
    STAMP_AT(0);
    const unsigned lbase = (unsigned)(size_t)(LAS unsigned char*)lds;

    unsigned epoch;
    asm volatile("global_load_dword %0, %1, %2 sc1" : "=v"(epoch) : "v"(0u), "s"(ws) : "memory");

    const u32x4 sx = make_srd(x, 8 * D_ * 2), s1 = make_srd(w1, D_ * F_ * 2), s3 = make_srd(w3, D_ * F_ * 2), s2 = make_srd(w2, D_ * F_ * 2);
    {
        const unsigned b = lane >> 3, c = lane & 7;
        const unsigned vx = b * 2048 + ((c ^ ((b >> 1) << 1)) << 4);
#pragma unroll
        for (int i = 0; i < 4; ++i) DMA16("", lbase + L_X + w * 4096 + i * 1024, vx, sx, w * 512 + i * 128);
    }
    {
        const unsigned p = lane >> 1;
        const unsigned row = 8 * ((p >> 2) & 3) + 4 * (p >> 4) + (p & 3);
        const unsigned vw = row * (F_ * 2) + (lane & 1) * 16 + id * 32;
#pragma unroll
        for (int s = 0; s < 8; ++s) {
            const unsigned so = (w * 256 + s * 32) * (F_ * 2);
            DMA16(W13_AUX, lbase + L_W13 + w * 16384 + s * 2048, vw, s1, so);
            DMA16(W13_AUX, lbase + L_W13 + w * 16384 + s * 2048 + 1024, vw, s3, so);
        }
    }
    {
        const unsigned r = lane >> 3, c = lane & 7;
        const unsigned v2 = r * 2048 + ((c ^ ((r >> 1) << 1)) << 4) + m * 128;
#pragma unroll
        for (int i = 0; i < 8; ++i) DMA16(W2_AUX, lbase + L_W2 + w * 8192 + i * 1024, v2, s2, (g * 256 + w * 64 + i * 8) * 2048);
    }
    STAMP_AT(1);

    f32x4 ag = {0.f, 0.f, 0.f, 0.f}, au = {0.f, 0.f, 0.f, 0.f};
    {
        const unsigned wa = lbase + L_W13 + w * 16384 + lane * 8;
        const unsigned b = lane & 7, oct = lane >> 4;
        const unsigned xa0 = lbase + L_X + w * 4096 + b * 128 + ((oct ^ ((b >> 1) << 1)) << 4);
        P1<0>::run(wa, xa0, xa0 ^ 64, ag, au);
    }
    asm volatile("" : "+v"(epoch));
    const unsigned tag = __builtin_amdgcn_readfirstlane(epoch) * 0x9E3779B9u + 0x7F4A7C15u;
    STAMP_AT(2);
    *(LAS f32x4*)(lbase + L_W13 + w * 16384 + lane * 16) = ag;
    *(LAS f32x4*)(lbase + L_W13 + w * 16384 + 1024 + lane * 16) = au;
    asm volatile("s_waitcnt lgkmcnt(0)\n\ts_barrier" ::: "memory");
    if (w == 0) {
        const unsigned pair = lane >> 5, e = (lane >> 4) & 1, sub = (lane >> 3) & 1, b = lane & 7;
        const unsigned ra = lbase + L_W13 + ((2 * sub + e) * 16 + b) * 16 + pair * 8;
        f32x2 gs = *(const LAS f32x2*)(ra), us = *(const LAS f32x2*)(ra + 1024);
#pragma unroll
        for (int i = 1; i < 4; ++i) { gs += *(const LAS f32x2*)(ra + i * 16384); us += *(const LAS f32x2*)(ra + i * 16384 + 1024); }
        bf16x2 hv;
#pragma unroll
        for (int j = 0; j < 2; ++j) {
            const float gv = bf_round(gs[j]), uv = bf_round(us[j]);
            const float sv = bf_round(gv * __builtin_amdgcn_rcpf(1.f + __expf(-gv)));
            hv[j] = (__bf16)(sv * uv);
        }
        gu64* hp = (gu64*)(ws + WS_H + (id >> 1) * 1024 + pair * 512 + (id & 1) * 256 + (lane & 31) * 8);
        __hip_atomic_store(hp, ((u64)tag << 32) | __builtin_bit_cast(unsigned, hv), RLX_AGENT);
    }
    STAMP_AT(3);

    f32x4 acc[4];
    {
        gu64* hp = (gu64*)(ws + WS_H + (g * 8 + w * 2) * 1024 + lane * 8);
        u64 v[4];
#pragma unroll
        for (int i = 0; i < 4; ++i) v[i] = __hip_atomic_load(hp + i * 64, RLX_AGENT);
        WAIT_VM(4);
        STAMP_AT(4);
        bf16x8 wf[2][4];
        {
            const unsigned hh = lane >> 5, e = (lane >> 4) & 1, q = (lane >> 2) & 3, p = lane & 3;
            const unsigned r = 4 * e + q;
            const unsigned a0 = lbase + L_W2 + w * 8192 + hh * 2048 + r * 128 + p * 8;
#pragma unroll
            for (int cb = 0; cb < 4; ++cb) {
                const unsigned a = a0 + ((cb ^ (r >> 1)) << 5);
#pragma unroll
                for (int ks = 0; ks < 2; ++ks) wf[ks][cb] = cat(tr_read(a + ks * 4096), tr_read(a + ks * 4096 + 1024));
            }
        }
        for (unsigned spins = 0;;) {
            bool ok = true;
#pragma unroll
            for (int i = 0; i < 4; ++i) ok &= (unsigned)(v[i] >> 32) == tag;
            if (__all(ok) || ++spins > SPIN_CAP) break;
#pragma unroll
            for (int i = 0; i < 4; ++i) v[i] = __hip_atomic_load(hp + i * 64, RLX_AGENT);
        }
        STAMP_AT(5);
#pragma unroll
        for (int cb = 0; cb < 4; ++cb) acc[cb] = f32x4{0.f, 0.f, 0.f, 0.f};
#pragma unroll
        for (int ks = 0; ks < 2; ++ks) {
            u32x4 hb;
            hb.x = (unsigned)v[2 * ks]; hb.y = (unsigned)v[2 * ks + 1];
            hb.z = __builtin_amdgcn_update_dpp(0u, hb.x, 0x128, 0xf, 0xf, true);
            hb.w = __builtin_amdgcn_update_dpp(0u, hb.y, 0x128, 0xf, 0xf, true);
            const bf16x8 hf = __builtin_bit_cast(bf16x8, hb);
#pragma unroll
            for (int cb = 0; cb < 4; ++cb) acc[cb] = __builtin_amdgcn_mfma_f32_16x16x32_bf16(wf[ks][cb], hf, acc[cb], 0, 0, 0);
        }
    }
#pragma unroll
    for (int cb = 0; cb < 4; ++cb) *(LAS f32x4*)(lbase + L_W13 + w * 16384 + 4096 + cb * 1024 + lane * 16) = acc[cb];
    asm volatile("s_waitcnt lgkmcnt(0)\n\ts_barrier" ::: "memory");
    {
        const unsigned b = lane >> 3, dp = lane & 7;
        const unsigned ra = lbase + L_W13 + 4096 + w * 1024 + ((dp >> 1) * 16 + b) * 16 + (dp & 1) * 8;
        f32x2 s = *(const LAS f32x2*)(ra);
#pragma unroll
        for (int i = 1; i < 4; ++i) s += *(const LAS f32x2*)(ra + i * 16384);
        gu64* pp = (gu64*)(ws + WS_P + ((m * 8 + b) * G_ + g) * 512 + w * 128 + dp * 8);
        const float sx0 = s.x, sy0 = s.y;
        __hip_atomic_store(pp, ((u64)tag << 32) | __float_as_uint(sx0), RLX_AGENT);
        __hip_atomic_store(pp + 8, ((u64)tag << 32) | __float_as_uint(sy0), RLX_AGENT);
    }
    STAMP_AT(6);

    if (g < 8) {
        const unsigned gq = lane >> 3, dp = lane & 7;
        const unsigned g2 = gq < 6 ? gq + 8 : gq;
        gu64* p0 = (gu64*)(ws + WS_P + ((m * 8 + g) * G_ + gq) * 512 + w * 128 + dp * 8);
        gu64* p1 = (gu64*)(ws + WS_P + ((m * 8 + g) * G_ + g2) * 512 + w * 128 + dp * 8);
        u64 v[4];
        for (unsigned spins = 0;;) {
            v[0] = __hip_atomic_load(p0, RLX_AGENT); v[1] = __hip_atomic_load(p0 + 8, RLX_AGENT);
            v[2] = __hip_atomic_load(p1, RLX_AGENT); v[3] = __hip_atomic_load(p1 + 8, RLX_AGENT);
            bool ok = true;
#pragma unroll
            for (int i = 0; i < 4; ++i) ok &= (unsigned)(v[i] >> 32) == tag;
            if (__all(ok) || ++spins > SPIN_CAP) break;
        }
        STAMP_AT(7);
        float s0 = __builtin_bit_cast(float, (unsigned)v[0]), s1v = __builtin_bit_cast(float, (unsigned)v[1]);
        if (gq < 6) { s0 += __builtin_bit_cast(float, (unsigned)v[2]); s1v += __builtin_bit_cast(float, (unsigned)v[3]); }
#pragma unroll
        for (int o = 8; o < 64; o <<= 1) { s0 += __shfl_xor(s0, o); s1v += __shfl_xor(s1v, o); }
        if (lane < 8) {
            bf16x2 o2; o2[0] = (__bf16)s0; o2[1] = (__bf16)s1v;
            *(GAS unsigned*)((GAS unsigned char*)out + (g * D_ + m * 64 + w * 16 + dp * 2) * 2) = __builtin_bit_cast(unsigned, o2);
        }
        if (id == 0 && tid == 0) __hip_atomic_store((gu32*)(ws + WS_EPOCH), tag, RLX_AGENT);
    }
    STAMP_AT(8);
    { const u64 until = st_[0] + 3000 + (st_[DIAG] ? 4 * (st_[DIAG] - st_[0]) : 0);
      while (__builtin_amdgcn_s_memrealtime() < until) __builtin_amdgcn_s_sleep(2); }
}

extern "C" void kernel_launch(void* const* d_in, const int* in_sizes, int n_in, void* d_out, int out_size, void* d_ws, size_t ws_size,
                              hipStream_t stream) {
    (void)hipFuncSetAttribute((const void*)swiglu_fused, hipFuncAttributeMaxDynamicSharedMemorySize, L_TOTAL);
    swiglu_fused<<<NWG, 256, L_TOTAL, stream>>>(d_in[0], d_in[1], d_in[2], d_in[3], d_out, (unsigned char*)d_ws);
}
```

```cpp
#define DIAG 0
#define KAMP 0
#include <hip/hip_runtime.h>
#include <stdint.h>

#define GAS __attribute__((address_space(1)))
#define LAS __attribute__((address_space(3)))
typedef float f32x4 __attribute__((ext_vector_type(4)));
typedef float f32x2 __attribute__((ext_vector_type(2)));
typedef short s16x4 __attribute__((ext_vector_type(4)));
typedef short s16x8 __attribute__((ext_vector_type(8)));
typedef __bf16 bf16x8 __attribute__((ext_vector_type(8)));
typedef __bf16 bf16x2 __attribute__((ext_vector_type(2)));
typedef unsigned u32x4 __attribute__((ext_vector_type(4)));
typedef unsigned u32x2 __attribute__((ext_vector_type(2)));
typedef unsigned long long u64;
typedef GAS u64 gu64;
typedef GAS unsigned gu32;
#define RLX_AGENT __ATOMIC_RELAXED, __HIP_MEMORY_SCOPE_AGENT

constexpr int D_ = 1024, F_ = 3584;
constexpr int NWG = 224, G_ = 14, M_ = 16;
constexpr int L_X = 0, L_W13 = 16384, L_W2 = 81920, L_TOTAL = 114688;
constexpr int WS_EPOCH = 0, WS_H = 4096, WS_P = WS_H + NWG * 512;
constexpr unsigned SPIN_CAP = 1u << 18;

#define W13_AUX ""
#define W2_AUX ""

#define STAMP_AT(i) asm volatile("s_memrealtime %0\n\ts_waitcnt lgkmcnt(0)" : "=s"(st_[i]) :: "memory")

__device__ __forceinline__ u32x4 make_srd(const void* p, unsigned bytes) {
    const u64 a = (u64)p;
    u32x4 r;
    r.x = (unsigned)a; r.y = (unsigned)(a >> 32) & 0xffffu; r.z = bytes; r.w = 0x00020000u;
    return r;
}
#define DMA16(AUX, ldsoff, voff, srd, soff) \
    asm volatile("s_mov_b32 m0, %0\n\ts_nop 0\n\tbuffer_load_dwordx4 %1, %2, %3 offen" AUX " lds" \
                 :: "s"(ldsoff), "v"(voff), "s"(srd), "s"(soff) : "memory")
#define WAIT_VM(n) asm volatile("s_waitcnt vmcnt(" #n ")" ::: "memory")

__device__ __forceinline__ s16x4 tr_read(unsigned addr) {
    typedef short v4i16_t __attribute__((ext_vector_type(4)));
    return __builtin_amdgcn_ds_read_tr16_b64_v4i16((LAS v4i16_t*)addr);
}
__device__ __forceinline__ bf16x8 cat(s16x4 a, s16x4 b) {
    s16x8 r = __builtin_shufflevector(a, b, 0, 1, 2, 3, 4, 5, 6, 7);
    return __builtin_bit_cast(bf16x8, r);
}
__device__ __forceinline__ float bf_round(float f) { return (float)(__bf16)f; }

template <int S> struct P1 {
    static __device__ __forceinline__ void run(unsigned wa, unsigned xa0, unsigned xa1, f32x4& ag, f32x4& au) {
        if constexpr (S == 0) WAIT_VM(22); else if constexpr (S == 1) WAIT_VM(20); else if constexpr (S == 2) WAIT_VM(18);
        else if constexpr (S == 3) WAIT_VM(16); else if constexpr (S == 4) WAIT_VM(14); else if constexpr (S == 5) WAIT_VM(12);
        else if constexpr (S == 6) WAIT_VM(10); else WAIT_VM(8);
        const bf16x8 xb = *(const LAS bf16x8*)(((S & 1) ? xa1 : xa0) + (S >> 1) * 1024);
        const bf16x8 wg = cat(tr_read(wa + S * 2048), tr_read(wa + S * 2048 + 512));
        const bf16x8 wu = cat(tr_read(wa + S * 2048 + 1024), tr_read(wa + S * 2048 + 1536));
        ag = __builtin_amdgcn_mfma_f32_16x16x32_bf16(wg, xb, ag, 0, 0, 0);
        au = __builtin_amdgcn_mfma_f32_16x16x32_bf16(wu, xb, au, 0, 0, 0);
        if constexpr (S < 7) P1<S + 1>::run(wa, xa0, xa1, ag, au);
    }
};

__global__ void __launch_bounds__(256, 1)
swiglu_fused(const void* __restrict__ x_, const void* __restrict__ w1_, const void* __restrict__ w3_, const void* __restrict__ w2_,
             void* __restrict__ out_, unsigned char* __restrict__ ws_) {
    u64 tA; asm volatile("s_memrealtime %0" : "=s"(tA) :: "memory");
    typedef u64 u64x4 __attribute__((ext_vector_type(4)));
    typedef u64 u64x2 __attribute__((ext_vector_type(2)));
    u64x4 ka4; u64x2 ka2;
    asm volatile("s_load_dwordx8 %0, %2, 0x0\n\ts_load_dwordx4 %1, %2, 0x20\n\ts_waitcnt lgkmcnt(0)" : "=&s"(ka4), "=&s"(ka2) : "s"(__builtin_amdgcn_kernarg_segment_ptr()) : "memory");
    const void* x = (const void*)ka4.x; const void* w1 = (const void*)ka4.y; const void* w3 = (const void*)ka4.z; const void* w2 = (const void*)ka4.w;
    void* out = (void*)ka2.x; unsigned char* ws = (unsigned char*)ka2.y;
    extern __shared__ __attribute__((aligned(16))) unsigned char lds[];
    const unsigned tid = threadIdx.x, lane = tid & 63;
    const unsigned w = __builtin_amdgcn_readfirstlane(tid >> 6);
    const unsigned bid = blockIdx.x;
    const unsigned id = (bid & 7) * 28 + (bid >> 3);
    const unsigned g = id >> 4, m = id & 15;
    u64 st_[10] = {0, 0, 0, 0, 0, 0, 0, 0, 0, 0};
    STAMP_AT(0);
    const unsigned lbase = (unsigned)(size_t)(LAS unsigned char*)lds;

    unsigned epoch;
    asm volatile("global_load_dword %0, %1, %2 sc1" : "=v"(epoch) : "v"(0u), "s"(ws) : "memory");

    const u32x4 sx = make_srd(x, 8 * D_ * 2), s1 = make_srd(w1, D_ * F_ * 2), s3 = make_srd(w3, D_ * F_ * 2), s2 = make_srd(w2, D_ * F_ * 2);
    {
        const unsigned b = lane >> 3, c = lane & 7;
        const unsigned vx = b * 2048 + ((c ^ ((b >> 1) << 1)) << 4);
#pragma unroll
        for (int i = 0; i < 4; ++i) DMA16("", lbase + L_X + w * 4096 + i * 1024, vx, sx, w * 512 + i * 128);
    }
    {
        const unsigned p = lane >> 1;
        const unsigned row = 8 * ((p >> 2) & 3) + 4 * (p >> 4) + (p & 3);
        const unsigned vw = row * (F_ * 2) + (lane & 1) * 16 + id * 32;
#pragma unroll
        for (int s = 0; s < 8; ++s) {
            const unsigned so = (w * 256 + s * 32) * (F_ * 2);
            DMA16(W13_AUX, lbase + L_W13 + w * 16384 + s * 2048, vw, s1, so);
            DMA16(W13_AUX, lbase + L_W13 + w * 16384 + s * 2048 + 1024, vw, s3, so);
        }
    }
    {
        const unsigned r = lane >> 3, c = lane & 7;
        const unsigned v2 = r * 2048 + ((c ^ ((r >> 1) << 1)) << 4) + m * 128;
#pragma unroll
        for (int i = 0; i < 8; ++i) DMA16(W2_AUX, lbase + L_W2 + w * 8192 + i * 1024, v2, s2, (g * 256 + w * 64 + i * 8) * 2048);
    }
    STAMP_AT(1);

    f32x4 ag = {0.f, 0.f, 0.f, 0.f}, au = {0.f, 0.f, 0.f, 0.f};
    {
        const unsigned wa = lbase + L_W13 + w * 16384 + lane * 8;
        const unsigned b = lane & 7, oct = lane >> 4;
        const unsigned xa0 = lbase + L_X + w * 4096 + b * 128 + ((oct ^ ((b >> 1) << 1)) << 4);
        P1<0>::run(wa, xa0, xa0 ^ 64, ag, au);
    }
    asm volatile("" : "+v"(epoch));
    const unsigned tag = __builtin_amdgcn_readfirstlane(epoch) * 0x9E3779B9u + 0x7F4A7C15u;
    STAMP_AT(2);
    *(LAS f32x4*)(lbase + L_W13 + w * 16384 + lane * 16) = ag;
    *(LAS f32x4*)(lbase + L_W13 + w * 16384 + 1024 + lane * 16) = au;
    asm volatile("s_waitcnt lgkmcnt(0)\n\ts_barrier" ::: "memory");
    if (w == 0) {
        const unsigned pair = lane >> 5, e = (lane >> 4) & 1, sub = (lane >> 3) & 1, b = lane & 7;
        const unsigned ra = lbase + L_W13 + ((2 * sub + e) * 16 + b) * 16 + pair * 8;
        f32x2 gs = *(const LAS f32x2*)(ra), us = *(const LAS f32x2*)(ra + 1024);
#pragma unroll
        for (int i = 1; i < 4; ++i) { gs += *(const LAS f32x2*)(ra + i * 16384); us += *(const LAS f32x2*)(ra + i * 16384 + 1024); }
        bf16x2 hv;
#pragma unroll
        for (int j = 0; j < 2; ++j) {
            const float gv = bf_round(gs[j]), uv = bf_round(us[j]);
            const float sv = bf_round(gv * __builtin_amdgcn_rcpf(1.f + __expf(-gv)));
            hv[j] = (__bf16)(sv * uv);
        }
        gu64* hp = (gu64*)(ws + WS_H + (id >> 1) * 1024 + pair * 512 + (id & 1) * 256 + (lane & 31) * 8);
        __hip_atomic_store(hp, ((u64)tag << 32) | __builtin_bit_cast(unsigned, hv), RLX_AGENT);
    }
    STAMP_AT(3);

    f32x4 acc[4];
    {
        gu64* hp = (gu64*)(ws + WS_H + (g * 8 + w * 2) * 1024 + lane * 8);
        u64 v[4];
#pragma unroll
        for (int i = 0; i < 4; ++i) v[i] = __hip_atomic_load(hp + i * 64, RLX_AGENT);
        WAIT_VM(4);
        STAMP_AT(4);
        bf16x8 wf[2][4];
        {
            const unsigned hh = lane >> 5, e = (lane >> 4) & 1, q = (lane >> 2) & 3, p = lane & 3;
            const unsigned r = 4 * e + q;
            const unsigned a0 = lbase + L_W2 + w * 8192 + hh * 2048 + r * 128 + p * 8;
#pragma unroll
            for (int cb = 0; cb < 4; ++cb) {
                const unsigned a = a0 + ((cb ^ (r >> 1)) << 5);
#pragma unroll
                for (int ks = 0; ks < 2; ++ks) wf[ks][cb] = cat(tr_read(a + ks * 4096), tr_read(a + ks * 4096 + 1024));
            }
        }
        for (unsigned spins = 0;;) {
            bool ok = true;
#pragma unroll
            for (int i = 0; i < 4; ++i) ok &= (unsigned)(v[i] >> 32) == tag;
            if (__all(ok) || ++spins > SPIN_CAP) break;
#pragma unroll
            for (int i = 0; i < 4; ++i) v[i] = __hip_atomic_load(hp + i * 64, RLX_AGENT);
        }
        STAMP_AT(5);
#pragma unroll
        for (int cb = 0; cb < 4; ++cb) acc[cb] = f32x4{0.f, 0.f, 0.f, 0.f};
#pragma unroll
        for (int ks = 0; ks < 2; ++ks) {
            u32x4 hb;
            hb.x = (unsigned)v[2 * ks]; hb.y = (unsigned)v[2 * ks + 1];
            hb.z = __builtin_amdgcn_update_dpp(0u, hb.x, 0x128, 0xf, 0xf, true);
            hb.w = __builtin_amdgcn_update_dpp(0u, hb.y, 0x128, 0xf, 0xf, true);
            const bf16x8 hf = __builtin_bit_cast(bf16x8, hb);
#pragma unroll
            for (int cb = 0; cb < 4; ++cb) acc[cb] = __builtin_amdgcn_mfma_f32_16x16x32_bf16(wf[ks][cb], hf, acc[cb], 0, 0, 0);
        }
    }
#pragma unroll
    for (int cb = 0; cb < 4; ++cb) *(LAS f32x4*)(lbase + L_W13 + w * 16384 + 4096 + cb * 1024 + lane * 16) = acc[cb];
    asm volatile("s_waitcnt lgkmcnt(0)\n\ts_barrier" ::: "memory");
    {
        const unsigned b = lane >> 3, dp = lane & 7;
        const unsigned ra = lbase + L_W13 + 4096 + w * 1024 + ((dp >> 1) * 16 + b) * 16 + (dp & 1) * 8;
        f32x2 s = *(const LAS f32x2*)(ra);
#pragma unroll
        for (int i = 1; i < 4; ++i) s += *(const LAS f32x2*)(ra + i * 16384);
        gu64* pp = (gu64*)(ws + WS_P + ((m * 8 + b) * G_ + g) * 512 + w * 128 + dp * 8);
        const float sx0 = s.x, sy0 = s.y;
        __hip_atomic_store(pp, ((u64)tag << 32) | __float_as_uint(sx0), RLX_AGENT);
        __hip_atomic_store(pp + 8, ((u64)tag << 32) | __float_as_uint(sy0), RLX_AGENT);
    }
    STAMP_AT(6);

    if (g < 8) {
        const unsigned gq = lane >> 3, dp = lane & 7;
        const unsigned g2 = gq < 6 ? gq + 8 : gq;
        gu64* p0 = (gu64*)(ws + WS_P + ((m * 8 + g) * G_ + gq) * 512 + w * 128 + dp * 8);
        gu64* p1 = (gu64*)(ws + WS_P + ((m * 8 + g) * G_ + g2) * 512 + w * 128 + dp * 8);
        u64 v[4];
        for (unsigned spins = 0;;) {
            v[0] = __hip_atomic_load(p0, RLX_AGENT); v[1] = __hip_atomic_load(p0 + 8, RLX_AGENT);
            v[2] = __hip_atomic_load(p1, RLX_AGENT); v[3] = __hip_atomic_load(p1 + 8, RLX_AGENT);
            bool ok = true;
#pragma unroll
            for (int i = 0; i < 4; ++i) ok &= (unsigned)(v[i] >> 32) == tag;
            if (__all(ok) || ++spins > SPIN_CAP) break;
        }
        STAMP_AT(7);
        float s0 = __builtin_bit_cast(float, (unsigned)v[0]), s1v = __builtin_bit_cast(float, (unsigned)v[1]);
        if (gq < 6) { s0 += __builtin_bit_cast(float, (unsigned)v[2]); s1v += __builtin_bit_cast(float, (unsigned)v[3]); }
#pragma unroll
        for (int o = 8; o < 64; o <<= 1) { s0 += __shfl_xor(s0, o); s1v += __shfl_xor(s1v, o); }
        if (lane < 8) {
            bf16x2 o2; o2[0] = (__bf16)s0; o2[1] = (__bf16)s1v;
            __hip_atomic_store((GAS unsigned*)((GAS unsigned char*)out + (g * D_ + m * 64 + w * 16 + dp * 2) * 2), __builtin_bit_cast(unsigned, o2), __ATOMIC_RELAXED, __HIP_MEMORY_SCOPE_SYSTEM);
        }
        if (id == 0 && tid == 0) __hip_atomic_store((gu32*)(ws + WS_EPOCH), tag, RLX_AGENT);
    }
    STAMP_AT(8);
    { const u64 until = tA + 3000 + KAMP * (st_[0] - tA);
      while (__builtin_amdgcn_s_memrealtime() < until) __builtin_amdgcn_s_sleep(2); }
}

extern "C" void kernel_launch(void* const* d_in, const int* in_sizes, int n_in, void* d_out, int out_size, void* d_ws, size_t ws_size,
                              hipStream_t stream) {
    (void)hipFuncSetAttribute((const void*)swiglu_fused, hipFuncAttributeMaxDynamicSharedMemorySize, L_TOTAL);
    swiglu_fused<<<NWG, 256, L_TOTAL, stream>>>(d_in[0], d_in[1], d_in[2], d_in[3], d_out, (unsigned char*)d_ws);
}
```

```cpp
#include <hip/hip_runtime.h>
constexpr int NWG = 256, L_TOTAL = 147456;

extern "C" __global__ void __launch_bounds__(256, 1) __attribute__((aligned(4096))) swiglu_fused(const void* x, const void* w1, const void* w3, const void* w2, void* out, void* ws) {
    asm volatile(
        "s_mov_b32 s16, %1\n\t"
        "s_mov_b64 s[0:1], %0\n\t"
        "v_mov_b32 v0, %2\n\t"
        ".Lstart_%=:\n\t"
        "s_load_dwordx2 s[2:3], s[0:1], 0x0\n\t"
        "s_load_dwordx8 s[4:11], s[0:1], 0x8\n\t"
        "s_load_dwordx2 s[12:13], s[0:1], 0x28\n\t"
        "v_readfirstlane_b32 s37, v0\n\t"
        "s_and_b32 s47, s16, 7\n\t"
        "s_lshr_b32 s36, s16, 3\n\t"
        "v_and_b32 v1, 63, v0\n\t"
        "v_lshrrev_b32 v9, 3, v1\n\t"
        "s_mul_i32 s40, s47, 896\n\t"
        "s_lshr_b32 s37, s37, 6\n\t"
        "s_mov_b32 s22, 0x700000\n\t"
        "s_mov_b32 s23, 0x20000\n\t"
        "v_and_b32 v4, 7, v1\n\t"
        "v_and_b32 v5, 6, v9\n\t"
        "v_xor_b32 v5, v5, v4\n\t"
        "v_lshlrev_b32 v5, 4, v5\n\t"
        "s_cmp_lt_u32 s36, 14\n\t"
        "s_cbranch_scc1 .Lprod_%=\n\t"
        "s_mov_b32 m0, 0x20000\n\t"
        "s_cmp_lt_u32 s36, 16\n\t"
        "s_cbranch_scc1 .Lspare_%=\n\t"
        "s_lshl_b32 s46, s36, 2\n\t"
        "s_add_u32 s46, s46, s37\n\t"
        "s_and_b32 s46, s46, 63\n\t"
        "s_bfe_u32 s38, s46, 0x20001\n\t"
        "s_lshl_b32 s38, s38, 5\n\t"
        "s_lshr_b32 s39, s46, 3\n\t"
        "s_mul_i32 s41, s37, 2\n\t"
        "s_add_u32 s39, s39, s41\n\t"
        "s_add_u32 s41, s39, 2\n\t"
        "s_and_b32 s41, s41, 7\n\t"
        "s_add_u32 s19, s38, s41\n\t"
        "s_add_u32 s19, s19, 24\n\t"
        "s_mul_i32 s19, s19, 0xe000\n\t"
        "s_add_u32 s19, s19, s40\n\t"
        "s_add_u32 s41, s39, 1\n\t"
        "s_and_b32 s41, s41, 7\n\t"
        "s_add_u32 s18, s38, s41\n\t"
        "s_add_u32 s18, s18, 16\n\t"
        "s_mul_i32 s18, s18, 0xe000\n\t"
        "s_add_u32 s18, s18, s40\n\t"
        "s_and_b32 s41, s39, 7\n\t"
        "s_add_u32 s38, s38, s41\n\t"
        "s_add_u32 s38, s38, 8\n\t"
        "s_mul_i32 s38, s38, 0xe000\n\t"
        "s_add_u32 s38, s38, s40\n\t"
        "v_mul_u32_u24 v3, 0x1c00, v9\n\t"
        "v_lshl_add_u32 v3, v4, 7, v3\n\t"
        "v_lshlrev_b32 v6, 7, v1\n\t"
        "s_lshl_b32 s43, s46, 8\n\t"
        "s_mov_b32 s41, 0x7f7f7f7f\n\t"
        "s_movk_i32 s26, 0x4000\n\t"
        "s_mov_b32 s27, s23\n\t"
        "s_mov_b64 exec, 3\n\t"
        "s_bitcmp1_b32 s37, 0\n\t"
        "s_waitcnt lgkmcnt(0)\n\t"
        "s_cselect_b64 s[20:21], s[6:7], s[4:5]\n\t"
        "s_mov_b64 s[24:25], s[2:3]\n\t"
        "buffer_load_dword v6, s[24:27], s43 offen lds\n\t"
        "s_mov_b32 exec_lo, s41\n\t"
        "s_mov_b32 exec_hi, s41\n\t"
        "buffer_load_dword v3, s[20:23], s38 offen lds\n\t"
        "s_mov_b64 s[60:61], s[12:13]\n\t"
        "s_mov_b32 s62, 0xc8000\n\t"
        "s_mov_b32 s63, s23\n\t"
        "buffer_load_dword v2, off, s[60:63], 0 sc1\n\t"
        "s_mov_b32 s38, s18\n\t"
        "s_barrier\n\t"
        "buffer_load_dword v3, s[20:23], s38 offen lds\n\t"
        "s_mov_b32 s38, s19\n\t"
        "s_lshr_b32 s39, s8, 7\n\t"
        "s_sub_u32 s39, 3, s39\n\t"
        "s_lshl_b32 s39, s39, 1\n\t"
        "s_or_b32 s39, s39, 2\n\t"
        "s_xor_b32 s39, s39, s46\n\t"
        "s_and_b32 s39, s39, 14\n\t"
        "s_cselect_b32 s38, s38, 0x40000000\n\t"
        "s_barrier\n\t"
        "buffer_load_dword v3, s[20:23], s38 offen lds\n\t"
        "s_mov_b64 exec, -1\n\t"
        "s_memrealtime s[92:93]\n\t"
        "s_branch .Lcbody_%=\n\t"
        ".Lspare_%=:\n\t"
        "s_cmp_lg_u32 s16, 116\n\t"
        "s_cbranch_scc1 .Lnoprobe_%=\n\t"
        "s_cmp_lg_u32 s37, 0\n\t"
        "s_cbranch_scc1 .Lprobed_%=\n\t"
        "s_mov_b32 s62, 0xd0000\n\t"
        "s_mov_b32 s63, s23\n\t"
        "s_mov_b32 s38, 0xce000\n\t"
        "s_xor_b32 s39, s38, 0x2000\n\t"
        "s_mov_b64 exec, 1\n\t"
        "s_waitcnt lgkmcnt(0)\n\t"
        "s_mov_b64 s[60:61], s[12:13]\n\t"
        "buffer_store_dword v1, off, s[60:63], s38 sc1\n\t"
        "buffer_store_dword v1, off, s[60:63], s39 sc1\n\t"
        "buffer_load_dword v2, off, s[60:63], 0 sc1\n\t"
        "s_waitcnt vmcnt(2)\n\t"
        "s_memtime s[88:89]\n\t"
        "s_waitcnt vmcnt(1)\n\t"
        "s_memtime s[90:91]\n\t"
        "s_waitcnt lgkmcnt(0)\n\t"
        "s_sub_u32 s90, s90, s88\n\t"
        "s_cmp_lt_u32 s90, 32\n\t"
        "s_cselect_b32 s90, 0x2000, 0\n\t"
        "s_waitcnt vmcnt(0)\n\t"
        "v_add_u32 v10, 0x9e3779b9, v2\n\t"
        "v_xor_b32 v10, s90, v10\n\t"
        "buffer_store_dword v10, off, s[60:63], 0 offset:128 sc1\n\t"
        "s_mov_b64 exec, -1\n\t"
        ".Lprobed_%=:\n\t"
        "s_barrier\n\t"
        ".Lnoprobe_%=:\n\t"
        "s_bitcmp1_b32 s36, 0\n\t"
        "s_waitcnt lgkmcnt(0)\n\t"
        "s_cselect_b64 s[20:21], s[6:7], s[4:5]\n\t"
        "s_mov_b32 s43, s40\n\t"
        "s_cmp_lg_u32 s37, 3\n\t"
        "s_cbranch_scc1 .Lnoic_%=\n\t"
        "s_getpc_b64 s[28:29]\n\t"
        ".Lpc_%=:\n\t"
        "s_sub_u32 s28, s28, .Lpc_%=-.Lstart_%=\n\t"
        "s_subb_u32 s29, s29, 0\n\t"
        "s_mov_b32 s30, .Lstop_%=-.Lstart_%=\n\t"
        "s_mov_b32 s31, s23\n\t"
        "v_lshlrev_b32 v6, 7, v1\n\t"
        "buffer_load_dword v6, s[28:31], 0 offen lds\n\t"
        ".Lnoic_%=:\n\t"
        "s_mov_b64 s[60:61], s[12:13]\n\t"
        "s_mov_b32 s62, 0xc8000\n\t"
        "s_mov_b32 s63, s23\n\t"
        "buffer_load_dword v2, off, s[60:63], 0 sc1\n\t"
        "s_add_u32 s41, s20, s40\n\t"
        "s_sub_u32 s41, 0x1ff, s41\n\t"
        "s_and_b32 s41, s41, 0x380\n\t"
        "s_cmp_eq_u32 s41, 0x380\n\t"
        "s_cbranch_scc1 .Lsbody_%=\n\t"
        "s_cmp_ge_u32 s37, 2\n\t"
        "s_cbranch_scc1 .Lsbody_%=\n\t"
        "s_add_u32 s40, s40, s41\n\t"
        "s_lshl_b32 s41, s37, 4\n\t"
        "s_add_u32 s41, s41, 1\n\t"
        "s_mul_i32 s41, s41, 0x38000\n\t"
        "s_add_u32 s40, s40, s41\n\t"
        "v_and_b32 v3, 31, v1\n\t"
        "v_and_b32 v6, 32, v1\n\t"
        "v_lshl_or_b32 v3, v6, 3, v3\n\t"
        "v_mul_u32_u24 v3, 0x1c00, v3\n\t"
        "buffer_load_dword v3, s[20:23], s40 offen lds\n\t"
        "s_branch .Lsbody_%=\n\t"
        ".Lprod_%=:\n\t"
        "v_and_b32 v3, 0xc0, v0\n\t"
        "v_lshlrev_b32 v3, 2, v3\n\t"
        "v_and_b32 v6, 4, v9\n\t"
        "v_lshl_or_b32 v3, v6, 1, v3\n\t"
        "v_and_or_b32 v3, v9, 3, v3\n\t"
        "v_mul_u32_u24 v3, 0x1c00, v3\n\t"
        "s_and_b32 s41, s36, 14\n\t"
        "s_lshl_b32 s41, s41, 6\n\t"
        "s_add_u32 s38, s40, s41\n\t"
        "v_add_u32 v10, v3, v5\n\t"
        "v_add_u32 v11, 0x6c00, v10\n\t"
        "v_add_u32 v12, 0x1b800, v10\n\t"
        "v_add_u32 v13, 0x22400, v10\n\t"
        "s_lshl_b32 s56, s37, 15\n\t"
        "s_mov_b32 m0, s56\n\t"
        "s_mov_b32 s46, s38\n\t"
        "s_bitcmp1_b32 s36, 0\n\t"
        "s_waitcnt lgkmcnt(0)\n\t"
        "s_cselect_b64 s[20:21], s[6:7], s[4:5]\n\t"
        "buffer_load_dwordx4 v10, s[20:23], s38 offen lds\n\t"
        "buffer_load_dwordx4 v11, s[20:23], s38 offen offset:1024 lds\n\t"
        "buffer_load_dwordx4 v12, s[20:23], s38 offen offset:2048 lds\n\t"
        "buffer_load_dwordx4 v13, s[20:23], s38 offen offset:3072 lds\n\t"
        "s_add_u32 s38, s38, 0x38000\n\t"
        "s_add_u32 m0, s56, 0x1000\n\t"
        "s_nop 0\n\t"
        "buffer_load_dwordx4 v10, s[20:23], s38 offen lds\n\t"
        "buffer_load_dwordx4 v11, s[20:23], s38 offen offset:1024 lds\n\t"
        "buffer_load_dwordx4 v12, s[20:23], s38 offen offset:2048 lds\n\t"
        "buffer_load_dwordx4 v13, s[20:23], s38 offen offset:3072 lds\n\t"
        "s_add_u32 s38, s38, 0x38000\n\t"
        "v_lshl_or_b32 v8, v9, 11, v5\n\t"
        "s_mov_b64 s[24:25], s[2:3]\n\t"
        "s_movk_i32 s26, 0x4000\n\t"
        "s_mov_b32 s27, s23\n\t"
        "s_mov_b64 s[60:61], s[12:13]\n\t"
        "s_mov_b32 s62, 0xc8000\n\t"
        "s_mov_b32 s63, s23\n\t"
        "buffer_load_dword v2, off, s[60:63], 0 sc1\n\t"
        "s_lshl_b32 s41, s37, 12\n\t"
        "s_add_u32 s43, s41, 0x20000\n\t"
        "s_mov_b32 m0, s43\n\t"
        "s_lshl_b32 s41, s37, 9\n\t"
        "buffer_load_dwordx4 v8, s[24:27], s41 offen lds\n\t"
        "s_add_u32 m0, m0, 0x400\n\t"
        "s_add_u32 s41, s41, 0x80\n\t"
        "buffer_load_dwordx4 v8, s[24:27], s41 offen lds\n\t"
        "s_add_u32 m0, m0, 0x400\n\t"
        "s_add_u32 s41, s41, 0x80\n\t"
        "buffer_load_dwordx4 v8, s[24:27], s41 offen lds\n\t"
        "s_add_u32 m0, m0, 0x400\n\t"
        "s_add_u32 s41, s41, 0x80\n\t"
        "buffer_load_dwordx4 v8, s[24:27], s41 offen lds\n\t"
        "v_and_b32 v3, 32, v1\n\t"
        "v_lshlrev_b32 v14, 6, v3\n\t"
        "v_and_b32 v3, 0x1c, v1\n\t"
        "v_lshl_or_b32 v14, v3, 5, v14\n\t"
        "v_and_b32 v3, 3, v1\n\t"
        "v_lshl_or_b32 v14, v3, 3, v14\n\t"
        "v_and_b32 v3, 0x18, v1\n\t"
        "v_lshl_or_b32 v14, v3, 2, v14\n\t"
        "v_or_b32 v14, s56, v14\n\t"
        "v_xor_b32 v15, 32, v14\n\t"
        "v_xor_b32 v112, 64, v14\n\t"
        "v_xor_b32 v113, 96, v14\n\t"
        "s_add_u32 m0, s56, 0x2000\n\t"
        "s_nop 0\n\t"
        "buffer_load_dwordx4 v10, s[20:23], s38 offen lds\n\t"
        "buffer_load_dwordx4 v11, s[20:23], s38 offen offset:1024 lds\n\t"
        "buffer_load_dwordx4 v12, s[20:23], s38 offen offset:2048 lds\n\t"
        "buffer_load_dwordx4 v13, s[20:23], s38 offen offset:3072 lds\n\t"
        "s_add_u32 s38, s38, 0x38000\n\t"
        "s_add_u32 m0, s56, 0x3000\n\t"
        "s_nop 0\n\t"
        "buffer_load_dwordx4 v10, s[20:23], s38 offen lds\n\t"
        "buffer_load_dwordx4 v11, s[20:23], s38 offen offset:1024 lds\n\t"
        "buffer_load_dwordx4 v12, s[20:23], s38 offen offset:2048 lds\n\t"
        "buffer_load_dwordx4 v13, s[20:23], s38 offen offset:3072 lds\n\t"
        "s_add_u32 s38, s38, 0x38000\n\t"
        "s_add_u32 m0, s56, 0x4000\n\t"
        "s_nop 0\n\t"
        "buffer_load_dwordx4 v10, s[20:23], s38 offen lds\n\t"
        "buffer_load_dwordx4 v11, s[20:23], s38 offen offset:1024 lds\n\t"
        "buffer_load_dwordx4 v12, s[20:23], s38 offen offset:2048 lds\n\t"
        "buffer_load_dwordx4 v13, s[20:23], s38 offen offset:3072 lds\n\t"
        "s_add_u32 s38, s38, 0x38000\n\t"
        "v_lshrrev_b32 v3, 4, v1\n\t"
        "v_and_b32 v5, 6, v1\n\t"
        "v_xor_b32 v3, v3, v5\n\t"
        "v_lshlrev_b32 v114, 7, v4\n\t"
        "v_lshl_or_b32 v114, v3, 4, v114\n\t"
        "v_or_b32 v114, s43, v114\n\t"
        "v_xor_b32 v115, 64, v114\n\t"
        "v_lshlrev_b32 v7, 4, v1\n\t"
        "v_or_b32 v116, s56, v7\n\t"
        "s_xor_b32 s41, s37, 1\n\t"
        "s_lshl_b32 s41, s41, 15\n\t"
        "s_lshl_b32 s44, s37, 10\n\t"
        "s_add_u32 s41, s41, s44\n\t"
        "v_add_u32 v117, s41, v7\n\t"
        "s_add_u32 s41, s44, 0x10000\n\t"
        "v_add_u32 v118, s41, v7\n\t"
        "s_mov_b32 s48, 0xff00ff00\n\t"
        "s_mov_b32 s49, 0xff00ff00\n\t"
        "s_mov_b64 s[28:29], s[8:9]\n\t"
        "s_mov_b64 s[30:31], s[22:23]\n\t"
        "s_xor_b32 s57, s47, 2\n\t"
        "s_and_b32 s57, s57, 6\n\t"
        "s_lshl_b32 s57, s57, 1\n\t"
        "s_bfe_u32 s41, s36, 0x20002\n\t"
        "s_add_u32 s57, s57, s41\n\t"
        "s_mul_i32 s57, s57, 224\n\t"
        "s_and_b32 s41, s36, 3\n\t"
        "s_lshl_b32 s41, s41, 2\n\t"
        "s_add_u32 s41, s41, s37\n\t"
        "s_add_u32 s57, s57, s41\n\t"
        "s_add_u32 s57, s57, 128\n\t"
        "s_lshl_b32 s57, s57, 11\n\t"
        "s_and_b32 s41, s47, 1\n\t"
        "s_lshl_b32 s41, s41, 10\n\t"
        "s_add_u32 s57, s57, s41\n\t"
        "v_lshlrev_b32 v119, 15, v9\n\t"
        "v_lshl_or_b32 v119, v4, 7, v119\n\t"
        "s_lshl_b32 s59, s16, 7\n\t"
        "s_add_u32 s59, s59, 0x1000\n\t"
        "s_cmp_eq_u32 s37, 3\n\t"
        "s_cselect_b32 s59, s59, 0x40000000\n\t"
        "s_lshl_b32 s46, s46, 5\n\t"
        "s_andn2_b32 s41, s46, 0x1fff\n\t"
        "s_add_u32 s46, s46, s41\n\t"
        "s_lshl_b32 s41, s37, 1\n\t"
        "s_and_b32 s44, s36, 1\n\t"
        "s_add_u32 s41, s41, s44\n\t"
        "s_lshl_b32 s41, s41, 10\n\t"
        "s_add_u32 s46, s46, s41\n\t"
        "s_mov_b32 s41, 0xc000\n\t"
        "s_cmp_ge_u32 s47, 4\n\t"
        "s_cselect_b32 s41, 0xfffd6000, s41\n\t"
        "s_add_u32 s46, s46, s41\n\t"
        "s_waitcnt vmcnt(12)\n\t"
        "ds_read_b128 v[16:19], v114 offset:0\n\t"
        "ds_read_b128 v[20:23], v115 offset:0\n\t"
        "ds_read_b128 v[24:27], v114 offset:1024\n\t"
        "ds_read_b128 v[28:31], v115 offset:1024\n\t"
        "ds_read_b128 v[32:35], v114 offset:2048\n\t"
        "ds_read_b128 v[36:39], v115 offset:2048\n\t"
        "ds_read_b128 v[40:43], v114 offset:3072\n\t"
        "ds_read_b128 v[44:47], v115 offset:3072\n\t"
        "v_readfirstlane_b32 s42, v2\n\t"
        "s_add_u32 s42, s42, 0x9e3779b9\n\t"
        "s_waitcnt vmcnt(21)\n\t"
        "ds_read_b64_tr_b16 v[48:49], v14 offset:0\n\t"
        "ds_read_b64_tr_b16 v[50:51], v14 offset:1024\n\t"
        "ds_read_b64_tr_b16 v[52:53], v15 offset:0\n\t"
        "ds_read_b64_tr_b16 v[54:55], v15 offset:1024\n\t"
        "ds_read_b64_tr_b16 v[56:57], v112 offset:0\n\t"
        "ds_read_b64_tr_b16 v[58:59], v112 offset:1024\n\t"
        "ds_read_b64_tr_b16 v[60:61], v113 offset:0\n\t"
        "ds_read_b64_tr_b16 v[62:63], v113 offset:1024\n\t"
        "s_add_u32 m0, s56, 0x5000\n\t"
        "s_nop 0\n\t"
        "buffer_load_dwordx4 v10, s[20:23], s38 offen lds\n\t"
        "buffer_load_dwordx4 v11, s[20:23], s38 offen offset:1024 lds\n\t"
        "buffer_load_dwordx4 v12, s[20:23], s38 offen offset:2048 lds\n\t"
        "buffer_load_dwordx4 v13, s[20:23], s38 offen offset:3072 lds\n\t"
        "s_add_u32 s38, s38, 0x38000\n\t"
        "s_waitcnt vmcnt(21)\n\t"
        "ds_read_b64_tr_b16 v[64:65], v14 offset:4096\n\t"
        "ds_read_b64_tr_b16 v[66:67], v14 offset:5120\n\t"
        "ds_read_b64_tr_b16 v[68:69], v15 offset:4096\n\t"
        "ds_read_b64_tr_b16 v[70:71], v15 offset:5120\n\t"
        "ds_read_b64_tr_b16 v[72:73], v112 offset:4096\n\t"
        "ds_read_b64_tr_b16 v[74:75], v112 offset:5120\n\t"
        "ds_read_b64_tr_b16 v[76:77], v113 offset:4096\n\t"
        "ds_read_b64_tr_b16 v[78:79], v113 offset:5120\n\t"
        "s_waitcnt lgkmcnt(14)\n\t"
        "v_mfma_f32_16x16x32_bf16 v[80:83], v[48:51], v[16:19], 0\n\t"
        "s_waitcnt lgkmcnt(12)\n\t"
        "v_mfma_f32_16x16x32_bf16 v[84:87], v[52:55], v[16:19], 0\n\t"
        "s_waitcnt lgkmcnt(10)\n\t"
        "v_mfma_f32_16x16x32_bf16 v[88:91], v[56:59], v[16:19], 0\n\t"
        "s_waitcnt lgkmcnt(8)\n\t"
        "v_mfma_f32_16x16x32_bf16 v[92:95], v[60:63], v[16:19], 0\n\t"
        "buffer_load_dword v120, off, s[60:63], 0 offset:128 sc1\n\t"
        "s_add_u32 m0, s56, 0x6000\n\t"
        "s_nop 0\n\t"
        "buffer_load_dwordx4 v10, s[20:23], s38 offen lds\n\t"
        "buffer_load_dwordx4 v11, s[20:23], s38 offen offset:1024 lds\n\t"
        "buffer_load_dwordx4 v12, s[20:23], s38 offen offset:2048 lds\n\t"
        "buffer_load_dwordx4 v13, s[20:23], s38 offen offset:3072 lds\n\t"
        "s_add_u32 s38, s38, 0x38000\n\t"
        "s_waitcnt vmcnt(17)\n\t"
        "ds_read_b64_tr_b16 v[48:49], v14 offset:8192\n\t"
        "ds_read_b64_tr_b16 v[50:51], v14 offset:9216\n\t"
        "ds_read_b64_tr_b16 v[52:53], v15 offset:8192\n\t"
        "ds_read_b64_tr_b16 v[54:55], v15 offset:9216\n\t"
        "ds_read_b64_tr_b16 v[56:57], v112 offset:8192\n\t"
        "ds_read_b64_tr_b16 v[58:59], v112 offset:9216\n\t"
        "ds_read_b64_tr_b16 v[60:61], v113 offset:8192\n\t"
        "ds_read_b64_tr_b16 v[62:63], v113 offset:9216\n\t"
        "s_waitcnt lgkmcnt(14)\n\t"
        "v_mfma_f32_16x16x32_bf16 v[80:83], v[64:67], v[20:23], v[80:83]\n\t"
        "s_waitcnt lgkmcnt(12)\n\t"
        "v_mfma_f32_16x16x32_bf16 v[84:87], v[68:71], v[20:23], v[84:87]\n\t"
        "s_waitcnt lgkmcnt(10)\n\t"
        "v_mfma_f32_16x16x32_bf16 v[88:91], v[72:75], v[20:23], v[88:91]\n\t"
        "s_waitcnt lgkmcnt(8)\n\t"
        "v_mfma_f32_16x16x32_bf16 v[92:95], v[76:79], v[20:23], v[92:95]\n\t"
        "s_add_u32 m0, s56, 0x7000\n\t"
        "s_nop 0\n\t"
        "buffer_load_dwordx4 v10, s[20:23], s38 offen lds\n\t"
        "buffer_load_dwordx4 v11, s[20:23], s38 offen offset:1024 lds\n\t"
        "buffer_load_dwordx4 v12, s[20:23], s38 offen offset:2048 lds\n\t"
        "buffer_load_dwordx4 v13, s[20:23], s38 offen offset:3072 lds\n\t"
        "s_barrier\n\t"
        "v_mov_b32 v2, s42\n\t"
        "s_mov_b64 exec, 1\n\t"
        "buffer_store_dword v2, off, s[60:63], s59 sc1\n\t"
        "s_mov_b64 exec, -1\n\t"
        "s_waitcnt vmcnt(18)\n\t"
        "ds_read_b64_tr_b16 v[64:65], v14 offset:12288\n\t"
        "ds_read_b64_tr_b16 v[66:67], v14 offset:13312\n\t"
        "ds_read_b64_tr_b16 v[68:69], v15 offset:12288\n\t"
        "ds_read_b64_tr_b16 v[70:71], v15 offset:13312\n\t"
        "ds_read_b64_tr_b16 v[72:73], v112 offset:12288\n\t"
        "ds_read_b64_tr_b16 v[74:75], v112 offset:13312\n\t"
        "ds_read_b64_tr_b16 v[76:77], v113 offset:12288\n\t"
        "ds_read_b64_tr_b16 v[78:79], v113 offset:13312\n\t"
        "s_waitcnt lgkmcnt(14)\n\t"
        "v_mfma_f32_16x16x32_bf16 v[80:83], v[48:51], v[24:27], v[80:83]\n\t"
        "s_waitcnt lgkmcnt(12)\n\t"
        "v_mfma_f32_16x16x32_bf16 v[84:87], v[52:55], v[24:27], v[84:87]\n\t"
        "s_waitcnt lgkmcnt(10)\n\t"
        "v_mfma_f32_16x16x32_bf16 v[88:91], v[56:59], v[24:27], v[88:91]\n\t"
        "s_waitcnt lgkmcnt(8)\n\t"
        "v_mfma_f32_16x16x32_bf16 v[92:95], v[60:63], v[24:27], v[92:95]\n\t"
        "s_waitcnt vmcnt(14)\n\t"
        "ds_read_b64_tr_b16 v[48:49], v14 offset:16384\n\t"
        "ds_read_b64_tr_b16 v[50:51], v14 offset:17408\n\t"
        "ds_read_b64_tr_b16 v[52:53], v15 offset:16384\n\t"
        "ds_read_b64_tr_b16 v[54:55], v15 offset:17408\n\t"
        "ds_read_b64_tr_b16 v[56:57], v112 offset:16384\n\t"
        "ds_read_b64_tr_b16 v[58:59], v112 offset:17408\n\t"
        "ds_read_b64_tr_b16 v[60:61], v113 offset:16384\n\t"
        "ds_read_b64_tr_b16 v[62:63], v113 offset:17408\n\t"
        "s_waitcnt lgkmcnt(14)\n\t"
        "v_mfma_f32_16x16x32_bf16 v[80:83], v[64:67], v[28:31], v[80:83]\n\t"
        "s_waitcnt lgkmcnt(12)\n\t"
        "v_mfma_f32_16x16x32_bf16 v[84:87], v[68:71], v[28:31], v[84:87]\n\t"
        "s_waitcnt lgkmcnt(10)\n\t"
        "v_mfma_f32_16x16x32_bf16 v[88:91], v[72:75], v[28:31], v[88:91]\n\t"
        "s_waitcnt lgkmcnt(8)\n\t"
        "v_mfma_f32_16x16x32_bf16 v[92:95], v[76:79], v[28:31], v[92:95]\n\t"
        "s_mov_b32 m0, s43\n\t"
        "s_nop 0\n\t"
        "s_mov_b32 exec_hi, 0xffff\n\t"
        "buffer_load_dword v119, s[28:31], s57 offen lds\n\t"
        "s_mov_b64 exec, -1\n\t"
        "s_waitcnt vmcnt(11)\n\t"
        "ds_read_b64_tr_b16 v[64:65], v14 offset:20480\n\t"
        "ds_read_b64_tr_b16 v[66:67], v14 offset:21504\n\t"
        "ds_read_b64_tr_b16 v[68:69], v15 offset:20480\n\t"
        "ds_read_b64_tr_b16 v[70:71], v15 offset:21504\n\t"
        "ds_read_b64_tr_b16 v[72:73], v112 offset:20480\n\t"
        "ds_read_b64_tr_b16 v[74:75], v112 offset:21504\n\t"
        "ds_read_b64_tr_b16 v[76:77], v113 offset:20480\n\t"
        "ds_read_b64_tr_b16 v[78:79], v113 offset:21504\n\t"
        "s_waitcnt lgkmcnt(14)\n\t"
        "v_mfma_f32_16x16x32_bf16 v[80:83], v[48:51], v[32:35], v[80:83]\n\t"
        "s_waitcnt lgkmcnt(12)\n\t"
        "v_mfma_f32_16x16x32_bf16 v[84:87], v[52:55], v[32:35], v[84:87]\n\t"
        "s_waitcnt lgkmcnt(10)\n\t"
        "v_mfma_f32_16x16x32_bf16 v[88:91], v[56:59], v[32:35], v[88:91]\n\t"
        "s_waitcnt lgkmcnt(8)\n\t"
        "v_mfma_f32_16x16x32_bf16 v[92:95], v[60:63], v[32:35], v[92:95]\n\t"
        "s_waitcnt vmcnt(6)\n\t"
        "ds_read_b64_tr_b16 v[48:49], v14 offset:24576\n\t"
        "ds_read_b64_tr_b16 v[50:51], v14 offset:25600\n\t"
        "ds_read_b64_tr_b16 v[52:53], v15 offset:24576\n\t"
        "ds_read_b64_tr_b16 v[54:55], v15 offset:25600\n\t"
        "ds_read_b64_tr_b16 v[56:57], v112 offset:24576\n\t"
        "ds_read_b64_tr_b16 v[58:59], v112 offset:25600\n\t"
        "ds_read_b64_tr_b16 v[60:61], v113 offset:24576\n\t"
        "ds_read_b64_tr_b16 v[62:63], v113 offset:25600\n\t"
        "s_mov_b32 s45, 0x40000\n\t"
        ".Lpip_%=:\n\t"
        "v_readfirstlane_b32 s52, v120\n\t"
        "s_xor_b32 s52, s52, s42\n\t"
        "s_andn2_b32 s41, s52, 0x2000\n\t"
        "s_cbranch_scc1 .Lpiwp_%=\n\t"
        "s_add_u32 s60, s60, s52\n\t"
        "s_addc_u32 s61, s61, 0\n\t"
        "s_waitcnt lgkmcnt(14)\n\t"
        "v_mfma_f32_16x16x32_bf16 v[80:83], v[64:67], v[36:39], v[80:83]\n\t"
        "s_waitcnt lgkmcnt(12)\n\t"
        "v_mfma_f32_16x16x32_bf16 v[84:87], v[68:71], v[36:39], v[84:87]\n\t"
        "s_waitcnt lgkmcnt(10)\n\t"
        "v_mfma_f32_16x16x32_bf16 v[88:91], v[72:75], v[36:39], v[88:91]\n\t"
        "s_waitcnt lgkmcnt(8)\n\t"
        "v_mfma_f32_16x16x32_bf16 v[92:95], v[76:79], v[36:39], v[92:95]\n\t"
        "s_waitcnt vmcnt(2)\n\t"
        "ds_read_b64_tr_b16 v[64:65], v14 offset:28672\n\t"
        "ds_read_b64_tr_b16 v[66:67], v14 offset:29696\n\t"
        "ds_read_b64_tr_b16 v[68:69], v15 offset:28672\n\t"
        "ds_read_b64_tr_b16 v[70:71], v15 offset:29696\n\t"
        "ds_read_b64_tr_b16 v[72:73], v112 offset:28672\n\t"
        "ds_read_b64_tr_b16 v[74:75], v112 offset:29696\n\t"
        "ds_read_b64_tr_b16 v[76:77], v113 offset:28672\n\t"
        "ds_read_b64_tr_b16 v[78:79], v113 offset:29696\n\t"
        "s_waitcnt lgkmcnt(14)\n\t"
        "v_mfma_f32_16x16x32_bf16 v[80:83], v[48:51], v[40:43], v[80:83]\n\t"
        "s_waitcnt lgkmcnt(12)\n\t"
        "v_mfma_f32_16x16x32_bf16 v[84:87], v[52:55], v[40:43], v[84:87]\n\t"
        "s_waitcnt lgkmcnt(10)\n\t"
        "v_mfma_f32_16x16x32_bf16 v[88:91], v[56:59], v[40:43], v[88:91]\n\t"
        "s_waitcnt lgkmcnt(8)\n\t"
        "v_mfma_f32_16x16x32_bf16 v[92:95], v[60:63], v[40:43], v[92:95]\n\t"
        "s_waitcnt lgkmcnt(6)\n\t"
        "v_mfma_f32_16x16x32_bf16 v[80:83], v[64:67], v[44:47], v[80:83]\n\t"
        "s_waitcnt lgkmcnt(4)\n\t"
        "v_mfma_f32_16x16x32_bf16 v[84:87], v[68:71], v[44:47], v[84:87]\n\t"
        "s_waitcnt lgkmcnt(2)\n\t"
        "v_mfma_f32_16x16x32_bf16 v[88:91], v[72:75], v[44:47], v[88:91]\n\t"
        "s_waitcnt lgkmcnt(0)\n\t"
        "v_mfma_f32_16x16x32_bf16 v[92:95], v[76:79], v[44:47], v[92:95]\n\t"
        "s_cmp_eq_u32 s37, 0\n\t"
        "s_nop 2\n\t"
        "v_cndmask_b32 v48, v80, v84, s[48:49]\n\t"
        "v_cndmask_b32 v49, v81, v85, s[48:49]\n\t"
        "v_cndmask_b32 v50, v82, v86, s[48:49]\n\t"
        "v_cndmask_b32 v51, v83, v87, s[48:49]\n\t"
        "s_cbranch_scc1 .Lw23_%=\n\t"
        "ds_write_b128 v116, v[48:51]\n\t"
        ".Lw23_%=:\n\t"
        "s_cmp_eq_u32 s37, 1\n\t"
        "v_cndmask_b32 v60, v88, v92, s[48:49]\n\t"
        "v_cndmask_b32 v61, v89, v93, s[48:49]\n\t"
        "v_cndmask_b32 v62, v90, v94, s[48:49]\n\t"
        "v_cndmask_b32 v63, v91, v95, s[48:49]\n\t"
        "s_cbranch_scc0 .Lwr23_%=\n\t"
        "v_mov_b32 v48, v60\n\t"
        "v_mov_b32 v49, v61\n\t"
        "v_mov_b32 v50, v62\n\t"
        "v_mov_b32 v51, v63\n\t"
        "s_branch .Lwdone_%=\n\t"
        ".Lwr23_%=:\n\t"
        "ds_write_b128 v116, v[60:63] offset:1024\n\t"
        ".Lwdone_%=:\n\t"
        "s_cmp_ge_u32 s37, 2\n\t"
        "s_waitcnt lgkmcnt(0)\n\t"
        "s_barrier\n\t"
        "s_cbranch_scc1 .Lreduce_%=\n\t"
        "ds_read_b128 v[52:55], v117\n\t"
        "ds_read_b128 v[56:59], v118\n\t"
        "ds_read_b128 v[60:63], v118 offset:32768\n\t"
        "v_mov_b32 v65, s42\n\t"
        "v_mov_b32 v67, s42\n\t"
        "s_bitcmp1_b32 s36, 0\n\t"
        "s_waitcnt lgkmcnt(2)\n\t"
        "v_pk_add_f32 v[48:49], v[48:49], v[52:53]\n\t"
        "v_pk_add_f32 v[50:51], v[50:51], v[54:55]\n\t"
        "s_waitcnt lgkmcnt(0)\n\t"
        "v_pk_add_f32 v[56:57], v[56:57], v[60:61]\n\t"
        "v_pk_add_f32 v[58:59], v[58:59], v[62:63]\n\t"
        "s_nop 0\n\t"
        "v_pk_add_f32 v[48:49], v[48:49], v[56:57]\n\t"
        "v_pk_add_f32 v[50:51], v[50:51], v[58:59]\n\t"
        "s_nop 0\n\t"
        "v_cvt_pk_bf16_f32 v64, v48, v49\n\t"
        "v_cvt_pk_bf16_f32 v66, v50, v51\n\t"
        "s_cbranch_scc1 .Lpub_%=\n\t"
        "v_lshlrev_b32 v48, 16, v64\n\t"
        "v_and_b32 v49, 0xffff0000, v64\n\t"
        "v_lshlrev_b32 v50, 16, v66\n\t"
        "v_and_b32 v51, 0xffff0000, v66\n\t"
        "v_mul_f32 v52, 0xbfb8aa3b, v48\n\t"
        "v_mul_f32 v53, 0xbfb8aa3b, v49\n\t"
        "v_mul_f32 v54, 0xbfb8aa3b, v50\n\t"
        "v_mul_f32 v55, 0xbfb8aa3b, v51\n\t"
        "v_exp_f32 v52, v52\n\t"
        "v_exp_f32 v53, v53\n\t"
        "v_exp_f32 v54, v54\n\t"
        "v_exp_f32 v55, v55\n\t"
        "s_nop 0\n\t"
        "v_add_f32 v52, 1.0, v52\n\t"
        "v_add_f32 v53, 1.0, v53\n\t"
        "v_add_f32 v54, 1.0, v54\n\t"
        "v_add_f32 v55, 1.0, v55\n\t"
        "v_rcp_f32 v52, v52\n\t"
        "v_rcp_f32 v53, v53\n\t"
        "v_rcp_f32 v54, v54\n\t"
        "v_rcp_f32 v55, v55\n\t"
        "s_nop 0\n\t"
        "v_mul_f32 v48, v48, v52\n\t"
        "v_mul_f32 v49, v49, v53\n\t"
        "v_mul_f32 v50, v50, v54\n\t"
        "v_mul_f32 v51, v51, v55\n\t"
        "v_cvt_pk_bf16_f32 v64, v48, v49\n\t"
        "v_cvt_pk_bf16_f32 v66, v50, v51\n\t"
        ".Lpub_%=:\n\t"
        "buffer_store_dwordx4 v[64:67], v7, s[60:63], s46 offen sc1\n\t"
        ".Lreduce_%=:\n\t"
        "s_andn2_b32 s41, 1, s47\n\t"
        "s_lshl_b32 s41, s41, 2\n\t"
        "s_lshr_b32 s38, s47, 1\n\t"
        "s_add_u32 s41, s41, s38\n\t"
        "s_lshl_b32 s41, s41, 3\n\t"
        "s_and_b32 s38, s36, 7\n\t"
        "s_add_u32 s41, s41, s38\n\t"
        "s_lshr_b32 s38, s36, 3\n\t"
        "s_mov_b64 s[24:25], s[10:11]\n\t"
        "s_movk_i32 s26, 0x4000\n\t"
        "s_mov_b32 s27, s23\n\t"
        "s_mov_b32 s45, 0x40000\n\t"
        "s_and_b32 s40, s41, 15\n\t"
        "s_and_b32 s46, s41, 32\n\t"
        "s_lshr_b32 s46, s46, 1\n\t"
        "s_add_u32 s40, s40, s46\n\t"
        "s_lshl_b32 s40, s40, 2\n\t"
        "s_add_u32 s40, s40, s38\n\t"
        "s_and_b32 s46, 4, s47\n\t"
        "s_lshr_b32 s46, s46, 1\n\t"
        "s_add_u32 s40, s40, s46\n\t"
        "s_lshl_b32 s40, s40, 12\n\t"
        "s_lshl_b32 s46, s37, 6\n\t"
        "s_add_u32 s40, s40, s46\n\t"
        "s_add_u32 s59, s40, 0x48000\n\t"
        "v_and_b32 v4, 3, v1\n\t"
        "v_lshrrev_b32 v3, 2, v1\n\t"
        "v_lshlrev_b32 v32, 8, v3\n\t"
        "v_lshl_or_b32 v32, v4, 4, v32\n\t"
        "s_lshl_b32 s38, s38, 13\n\t"
        "s_lshl_b32 s41, s41, 5\n\t"
        "s_add_u32 s38, s38, s41\n\t"
        "s_lshl_b32 s39, s37, 3\n\t"
        "s_add_u32 s38, s38, s39\n\t"
        "v_lshl_add_u32 v33, v4, 1, s38\n\t"
        "v_and_b32 v3, 0x30, v1\n\t"
        "v_lshl_add_u32 v33, v3, 7, v33\n\t"
        "v_mov_b32 v35, 0x07060302\n\t"
        "v_mov_b32 v36, 0x0504030c\n\t"
        "s_mov_b32 exec_lo, 0x00010001\n\t"
        "s_mov_b32 exec_hi, 0x00010001\n\t"
        "buffer_load_ushort v37, v33, s[24:27], 0 offen\n\t"
        "s_mov_b64 exec, -1\n\t"
        "s_lshl_b32 s41, s16, 2\n\t"
        "s_add_u32 s41, s41, s37\n\t"
        "s_cmp_eq_u32 s41, 480\n\t"
        "s_cbranch_scc1 .Lpoll2_%=\n\t"
        "s_sleep 34\n\t"
        ".Lpoll2_%=:\n\t"
        "buffer_load_dwordx4 v[16:19], v32, s[60:63], s59 offen sc1\n\t"
        "s_sub_u32 s45, s45, 1\n\t"
        "s_cbranch_scc1 .Lfin_%=\n\t"
        "s_waitcnt vmcnt(0)\n\t"
        "v_perm_b32 v3, v19, v17, v35\n\t"
        "v_cmp_ne_u32 vcc, s42, v3\n\t"
        "s_mov_b64 exec, vcc\n\t"
        "s_cbranch_vccnz .Lpoll2_%=\n\t"
        "s_mov_b64 exec, -1\n\t"
        "v_lshlrev_b32 v24, 8, v16\n\t"
        "v_perm_b32 v25, v17, v16, v36\n\t"
        "v_lshlrev_b32 v26, 8, v18\n\t"
        "v_perm_b32 v27, v19, v18, v36\n\t"
        "s_nop 0\n\t"
        "v_permlane32_swap_b32 v24, v26\n\t"
        "v_permlane32_swap_b32 v25, v27\n\t"
        "v_add_f32 v24, v24, v26\n\t"
        "v_add_f32 v25, v25, v27\n\t"
        "s_nop 1\n\t"
        "v_permlane16_swap_b32 v24, v25\n\t"
        "v_add_f32 v24, v24, v25\n\t"
        "s_nop 1\n\t"
        "v_add_f32_dpp v24, v24, v24 row_ror:8 row_mask:0xf bank_mask:0xf\n\t"
        "s_nop 1\n\t"
        "v_add_f32_dpp v24, v24, v24 row_ror:4 row_mask:0xf bank_mask:0xf\n\t"
        "s_mov_b32 exec_lo, 0x000f000f\n\t"
        "s_mov_b32 exec_hi, 0x000f000f\n\t"
        "v_cvt_pk_bf16_f32 v24, v24, v24\n\t"
        "buffer_store_short v24, v33, s[24:27], 0 offen\n\t"
        ".Lfin_%=:\n\t"
        "s_endpgm\n\t"
        ".p2align 12\n\t"
        ".Lcbody_%=:\n\t"
        "s_xor_b32 s43, s47, 2\n\t"
        "s_and_b32 s43, s43, 6\n\t"
        "s_lshl_b32 s43, s43, 1\n\t"
        "s_bfe_u32 s41, s36, 0x20002\n\t"
        "s_add_u32 s43, s43, s41\n\t"
        "s_and_b32 s44, s47, 1\n\t"
        "s_lshl_b32 s44, s44, 2\n\t"
        "s_and_b32 s41, s36, 3\n\t"
        "s_add_u32 s44, s44, s41\n\t"
        "v_lshl_or_b32 v8, v9, 11, v5\n\t"
        "s_mov_b64 s[28:29], s[8:9]\n\t"
        "s_mov_b64 s[30:31], s[22:23]\n\t"
        "s_lshr_b32 s38, s37, 1\n\t"
        "s_and_b32 s39, s37, 1\n\t"
        "s_mul_i32 s57, s43, 28\n\t"
        "s_add_u32 s57, s57, s38\n\t"
        "s_lshl_b32 s57, s57, 14\n\t"
        "s_lshl_b32 s41, s44, 1\n\t"
        "s_add_u32 s41, s41, s39\n\t"
        "s_lshl_b32 s41, s41, 7\n\t"
        "s_add_u32 s57, s57, s41\n\t"
        "s_mul_i32 s41, s39, 28\n\t"
        "s_add_u32 s41, s41, s38\n\t"
        "s_lshl_b32 s56, s41, 10\n\t"
        "s_lshl_b32 s59, s16, 7\n\t"
        "s_add_u32 s59, s59, 0x1000\n\t"
        "s_cmp_eq_u32 s37, 0\n\t"
        "s_cselect_b32 s59, s59, 0x40000000\n\t"
        "v_and_b32 v3, 32, v1\n\t"
        "v_lshlrev_b32 v10, 6, v3\n\t"
        "v_and_b32 v3, 0x1c, v1\n\t"
        "v_lshl_or_b32 v10, v3, 5, v10\n\t"
        "v_and_b32 v3, 3, v1\n\t"
        "v_lshl_or_b32 v10, v3, 3, v10\n\t"
        "v_and_b32 v3, 0x18, v1\n\t"
        "v_lshl_or_b32 v10, v3, 2, v10\n\t"
        "s_mul_i32 s41, s38, 28672\n\t"
        "v_add_u32 v10, s41, v10\n\t"
        "s_lshl_b32 s41, s39, 6\n\t"
        "v_xor_b32 v10, s41, v10\n\t"
        "v_xor_b32 v11, 32, v10\n\t"
        "v_lshlrev_b32 v7, 4, v1\n\t"
        "s_and_b32 s58, s43, 7\n\t"
        "s_mul_i32 s58, s58, 7\n\t"
        "s_add_u32 s58, s58, s37\n\t"
        "s_lshl_b32 s58, s58, 11\n\t"
        "s_andn2_b32 s41, s58, 0x1fff\n\t"
        "s_add_u32 s58, s58, s41\n\t"
        "s_and_b32 s41, 4, s47\n\t"
        "s_lshl_b32 s41, s41, 11\n\t"
        "s_add_u32 s58, s58, s41\n\t"
        "s_add_u32 s58, s58, 0xc000\n\t"
        "s_add_u32 s17, s58, 0x4000\n\t"
        "v_and_b32 v3, 0x30, v1\n\t"
        "v_lshlrev_b32 v13, 3, v3\n\t"
        "v_lshl_or_b32 v13, v4, 4, v13\n\t"
        "v_add_u32 v13, 0xe000, v13\n\t"
        "v_and_b32 v5, 8, v1\n\t"
        "v_lshlrev_b32 v12, 5, v5\n\t"
        "v_and_b32 v5, 16, v1\n\t"
        "v_lshl_or_b32 v12, v5, 3, v12\n\t"
        "v_lshl_or_b32 v12, v4, 4, v12\n\t"
        "v_lshrrev_b32 v5, 2, v1\n\t"
        "v_and_or_b32 v12, v5, 8, v12\n\t"
        "s_lshl_b32 s41, s37, 9\n\t"
        "s_add_u32 s41, s41, 0xe000\n\t"
        "v_add_u32 v12, s41, v12\n\t"
        "v_lshlrev_b32 v14, 8, v3\n\t"
        "v_and_b32 v3, 0x2000, v14\n\t"
        "v_add_u32 v14, v14, v3\n\t"
        "v_and_b32 v3, 15, v1\n\t"
        "v_lshl_or_b32 v14, v3, 4, v14\n\t"
        "s_and_b32 s40, s47, 1\n\t"
        "s_lshl_b32 s40, s40, 1\n\t"
        "s_and_b32 s41, s44, 1\n\t"
        "s_add_u32 s40, s40, s41\n\t"
        "s_lshl_b32 s40, s40, 2\n\t"
        "s_add_u32 s40, s40, s37\n\t"
        "s_lshl_b32 s40, s40, 15\n\t"
        "s_and_b32 s41, s44, 2\n\t"
        "s_lshl_b32 s41, s41, 12\n\t"
        "s_add_u32 s40, s40, s41\n\t"
        "s_lshl_b32 s41, s43, 8\n\t"
        "s_add_u32 s40, s40, s41\n\t"
        "s_add_u32 s40, s40, 0x48000\n\t"
        "v_mov_b32 v121, 0x05030201\n\t"
        "v_mov_b32 v122, 0x05040302\n\t"
        "v_mov_b32 v123, 0x07060302\n\t"
        "s_waitcnt vmcnt(2)\n\t"
        "v_readfirstlane_b32 s42, v2\n\t"
        "s_add_u32 s42, s42, 0x9e3779b9\n\t"
        "s_barrier\n\t"
        "v_mov_b32 v2, s42\n\t"
        "s_mov_b64 exec, 1\n\t"
        "buffer_store_dword v2, off, s[60:63], s59 sc1\n\t"
        "s_mov_b64 exec, -1\n\t"
        "s_mov_b32 m0, s56\n\t"
        "v_mov_b32 v120, s42\n\t"
        "buffer_load_dwordx4 v8, s[28:31], s57 offen lds\n\t"
        "s_add_u32 s57, s57, 0x7800\n\t"
        "buffer_load_dwordx4 v8, s[28:31], s57 offen offset:2048 lds\n\t"
        "s_add_u32 m0, m0, 0x1000\n\t"
        "s_add_u32 s57, s57, 0x8800\n\t"
        "buffer_load_dword v15, off, s[60:63], 0 offset:128 sc1\n\t"
        "buffer_load_dwordx4 v8, s[28:31], s57 offen lds\n\t"
        "s_add_u32 s57, s57, 0x7800\n\t"
        "buffer_load_dwordx4 v8, s[28:31], s57 offen offset:2048 lds\n\t"
        "s_add_u32 m0, m0, 0x1000\n\t"
        "s_add_u32 s57, s57, 0x8800\n\t"
        "buffer_load_dwordx4 v8, s[28:31], s57 offen lds\n\t"
        "s_add_u32 s57, s57, 0x7800\n\t"
        "buffer_load_dwordx4 v8, s[28:31], s57 offen offset:2048 lds\n\t"
        "s_add_u32 m0, m0, 0x1000\n\t"
        "s_add_u32 s57, s57, 0x8800\n\t"
        "buffer_load_dwordx4 v8, s[28:31], s57 offen lds\n\t"
        "s_add_u32 s57, s57, 0x7800\n\t"
        "buffer_load_dwordx4 v8, s[28:31], s57 offen offset:2048 lds\n\t"
        "s_add_u32 m0, m0, 0x1000\n\t"
        "s_add_u32 s57, s57, 0x8800\n\t"
        "buffer_load_dwordx4 v8, s[28:31], s57 offen lds\n\t"
        "s_add_u32 s57, s57, 0x7800\n\t"
        "buffer_load_dwordx4 v8, s[28:31], s57 offen offset:2048 lds\n\t"
        "s_add_u32 m0, m0, 0x1000\n\t"
        "s_add_u32 s57, s57, 0x8800\n\t"
        "buffer_load_dwordx4 v8, s[28:31], s57 offen lds\n\t"
        "s_add_u32 s57, s57, 0x7800\n\t"
        "buffer_load_dwordx4 v8, s[28:31], s57 offen offset:2048 lds\n\t"
        "s_add_u32 m0, m0, 0x1000\n\t"
        "s_add_u32 s57, s57, 0x8800\n\t"
        "buffer_load_dwordx4 v8, s[28:31], s57 offen lds\n\t"
        "s_add_u32 s57, s57, 0x7800\n\t"
        "buffer_load_dwordx4 v8, s[28:31], s57 offen offset:2048 lds\n\t"
        "s_waitcnt vmcnt(12)\n\t"
        "s_mov_b32 s45, 0x40000\n\t"
        ".Lpic_%=:\n\t"
        "v_readfirstlane_b32 s52, v15\n\t"
        "s_xor_b32 s52, s52, s42\n\t"
        "s_andn2_b32 s41, s52, 0x2000\n\t"
        "s_cbranch_scc1 .Lpiwc_%=\n\t"
        "s_add_u32 s60, s60, s52\n\t"
        "s_addc_u32 s61, s61, 0\n\t"
        "s_mov_b32 s45, 0x40000\n\t"
        ".Lht_%=:\n\t"
        "s_memrealtime s[94:95]\n\t"
        "s_getreg_b32 s41, hwreg(7, 0, 4)\n\t"
        "s_cmp_le_u32 s41, 2\n\t"
        "s_cbranch_scc1 .Lhgo_%=\n\t"
        "s_waitcnt lgkmcnt(0)\n\t"
        "s_sub_u32 s41, s94, s92\n\t"
        "s_cmp_lt_u32 s41, 355\n\t"
        "s_cbranch_scc1 .Lht_%=\n\t"
        ".Lhgo_%=:\n\t"
        "s_cmp_lt_u32 s37, 3\n\t"
        "s_cselect_b32 s54, 0, 2\n\t"
        "buffer_load_dwordx4 v[72:75], v7, s[60:63], s58 offen offset:0 sc1\n\t"
        "buffer_load_dwordx4 v[76:79], v7, s[60:63], s58 offen offset:1024 sc1\n\t"
        "s_cbranch_scc0 .Lone_%=\n\t"
        "buffer_load_dwordx4 v[80:83], v7, s[60:63], s17 offen offset:0 sc1\n\t"
        "buffer_load_dwordx4 v[84:87], v7, s[60:63], s17 offen offset:1024 sc1\n\t"
        "s_waitcnt vmcnt(6)\n\t"
        "s_branch .Ltwo_%=\n\t"
        ".Lone_%=:\n\t"
        "s_waitcnt vmcnt(4)\n\t"
        ".Ltwo_%=:\n\t"
        "s_barrier\n\t"
        "ds_read_b64_tr_b16 v[16:17], v10 offset:0\n\t"
        "ds_read_b64_tr_b16 v[18:19], v10 offset:1024\n\t"
        "ds_read_b64_tr_b16 v[20:21], v11 offset:0\n\t"
        "ds_read_b64_tr_b16 v[22:23], v11 offset:1024\n\t"
        "ds_read_b64_tr_b16 v[24:25], v10 offset:4096\n\t"
        "ds_read_b64_tr_b16 v[26:27], v10 offset:5120\n\t"
        "ds_read_b64_tr_b16 v[28:29], v11 offset:4096\n\t"
        "ds_read_b64_tr_b16 v[30:31], v11 offset:5120\n\t"
        "ds_read_b64_tr_b16 v[32:33], v10 offset:8192\n\t"
        "ds_read_b64_tr_b16 v[34:35], v10 offset:9216\n\t"
        "ds_read_b64_tr_b16 v[36:37], v11 offset:8192\n\t"
        "ds_read_b64_tr_b16 v[38:39], v11 offset:9216\n\t"
        "ds_read_b64_tr_b16 v[40:41], v10 offset:12288\n\t"
        "ds_read_b64_tr_b16 v[42:43], v10 offset:13312\n\t"
        "ds_read_b64_tr_b16 v[44:45], v11 offset:12288\n\t"
        "ds_read_b64_tr_b16 v[46:47], v11 offset:13312\n\t"
        "ds_read_b64_tr_b16 v[48:49], v10 offset:16384\n\t"
        "ds_read_b64_tr_b16 v[50:51], v10 offset:17408\n\t"
        "ds_read_b64_tr_b16 v[52:53], v11 offset:16384\n\t"
        "ds_read_b64_tr_b16 v[54:55], v11 offset:17408\n\t"
        "ds_read_b64_tr_b16 v[56:57], v10 offset:20480\n\t"
        "ds_read_b64_tr_b16 v[58:59], v10 offset:21504\n\t"
        "ds_read_b64_tr_b16 v[60:61], v11 offset:20480\n\t"
        "ds_read_b64_tr_b16 v[62:63], v11 offset:21504\n\t"
        "s_mov_b32 s48, 0\n\t"
        "s_mov_b32 s49, -1\n\t"
        "s_cmp_eq_u32 s37, 3\n\t"
        "s_cbranch_scc1 .Lrest_%=\n\t"
        "s_waitcnt vmcnt(2)\n\t"
        "v_bitop3_b32 v3, v73, v75, s42 bitop3:0x7e\n\t"
        "v_bitop3_b32 v5, v77, v79, s42 bitop3:0x7e\n\t"
        "v_or_b32 v3, v3, v5\n\t"
        "v_cmp_ne_u32 vcc, 0, v3\n\t"
        "s_cbranch_vccnz .Lrest_%=\n\t"
        "v_lshlrev_b32 v88, 16, v72\n\t"
        "v_and_b32 v89, 0xffff0000, v72\n\t"
        "v_lshlrev_b32 v90, 16, v76\n\t"
        "v_and_b32 v91, 0xffff0000, v76\n\t"
        "v_lshlrev_b32 v92, 16, v74\n\t"
        "v_and_b32 v93, 0xffff0000, v74\n\t"
        "v_lshlrev_b32 v94, 16, v78\n\t"
        "v_and_b32 v95, 0xffff0000, v78\n\t"
        "v_pk_mul_f32 v[88:89], v[88:89], v[90:91]\n\t"
        "v_pk_mul_f32 v[92:93], v[92:93], v[94:95]\n\t"
        "s_bitset1_b32 s54, 0\n\t"
        "v_cvt_pk_bf16_f32 v72, v88, v89\n\t"
        "v_cvt_pk_bf16_f32 v73, v92, v93\n\t"
        "ds_write_b64 v12, v[72:73] offset:0\n\t"
        "s_waitcnt vmcnt(0)\n\t"
        "v_bitop3_b32 v3, v81, v83, s42 bitop3:0x7e\n\t"
        "v_bitop3_b32 v5, v85, v87, s42 bitop3:0x7e\n\t"
        "v_or_b32 v3, v3, v5\n\t"
        "v_cmp_ne_u32 vcc, 0, v3\n\t"
        "s_cbranch_vccnz .Lpoll1_%=\n\t"
        "v_lshlrev_b32 v88, 16, v80\n\t"
        "v_and_b32 v89, 0xffff0000, v80\n\t"
        "v_lshlrev_b32 v90, 16, v84\n\t"
        "v_and_b32 v91, 0xffff0000, v84\n\t"
        "v_lshlrev_b32 v92, 16, v82\n\t"
        "v_and_b32 v93, 0xffff0000, v82\n\t"
        "v_lshlrev_b32 v94, 16, v86\n\t"
        "v_and_b32 v95, 0xffff0000, v86\n\t"
        "v_pk_mul_f32 v[88:89], v[88:89], v[90:91]\n\t"
        "v_pk_mul_f32 v[92:93], v[92:93], v[94:95]\n\t"
        "s_bitset1_b32 s54, 1\n\t"
        "v_cvt_pk_bf16_f32 v80, v88, v89\n\t"
        "v_cvt_pk_bf16_f32 v81, v92, v93\n\t"
        "ds_write_b64 v12, v[80:81] offset:2048\n\t"
        "s_branch .Lgoth2_%=\n\t"
        ".Lrest_%=:\n\t"
        ".Lpoll1_%=:\n\t"
        "s_waitcnt vmcnt(0)\n\t"
        ".Lchk_%=:\n\t"
        "s_bitcmp1_b32 s54, 0\n\t"
        "s_cbranch_scc1 .Lnext0_%=\n\t"
        "v_bitop3_b32 v3, v73, v75, s42 bitop3:0x7e\n\t"
        "v_bitop3_b32 v5, v77, v79, s42 bitop3:0x7e\n\t"
        "v_cmp_ne_u32 s[48:49], 0, v3\n\t"
        "v_cmp_ne_u32 s[50:51], 0, v5\n\t"
        "s_or_b64 vcc, s[48:49], s[50:51]\n\t"
        "s_cbranch_scc0 .Lready0_%=\n\t"
        "s_mov_b64 exec, s[48:49]\n\t"
        "s_cbranch_execz .Lsk00_%=\n\t"
        "buffer_load_dwordx4 v[72:75], v7, s[60:63], s58 offen offset:0 sc1\n\t"
        ".Lsk00_%=:\n\t"
        "s_mov_b64 exec, s[50:51]\n\t"
        "s_cbranch_execz .Lsk01_%=\n\t"
        "buffer_load_dwordx4 v[76:79], v7, s[60:63], s58 offen offset:1024 sc1\n\t"
        ".Lsk01_%=:\n\t"
        "s_mov_b64 exec, -1\n\t"
        "s_branch .Lnext0_%=\n\t"
        ".Lready0_%=:\n\t"
        "v_lshlrev_b32 v88, 16, v72\n\t"
        "v_and_b32 v89, 0xffff0000, v72\n\t"
        "v_lshlrev_b32 v90, 16, v76\n\t"
        "v_and_b32 v91, 0xffff0000, v76\n\t"
        "v_lshlrev_b32 v92, 16, v74\n\t"
        "v_and_b32 v93, 0xffff0000, v74\n\t"
        "v_lshlrev_b32 v94, 16, v78\n\t"
        "v_and_b32 v95, 0xffff0000, v78\n\t"
        "v_pk_mul_f32 v[88:89], v[88:89], v[90:91]\n\t"
        "v_pk_mul_f32 v[92:93], v[92:93], v[94:95]\n\t"
        "s_bitset1_b32 s54, 0\n\t"
        "v_cvt_pk_bf16_f32 v72, v88, v89\n\t"
        "v_cvt_pk_bf16_f32 v73, v92, v93\n\t"
        "ds_write_b64 v12, v[72:73] offset:0\n\t"
        ".Lnext0_%=:\n\t"
        "s_bitcmp1_b32 s54, 1\n\t"
        "s_cbranch_scc1 .Lnext1_%=\n\t"
        "v_bitop3_b32 v3, v81, v83, s42 bitop3:0x7e\n\t"
        "v_bitop3_b32 v5, v85, v87, s42 bitop3:0x7e\n\t"
        "v_cmp_ne_u32 s[48:49], 0, v3\n\t"
        "v_cmp_ne_u32 s[50:51], 0, v5\n\t"
        "s_or_b64 vcc, s[48:49], s[50:51]\n\t"
        "s_cbranch_scc0 .Lready1_%=\n\t"
        "s_mov_b64 exec, s[48:49]\n\t"
        "s_cbranch_execz .Lsk10_%=\n\t"
        "buffer_load_dwordx4 v[80:83], v7, s[60:63], s17 offen offset:0 sc1\n\t"
        ".Lsk10_%=:\n\t"
        "s_mov_b64 exec, s[50:51]\n\t"
        "s_cbranch_execz .Lsk11_%=\n\t"
        "buffer_load_dwordx4 v[84:87], v7, s[60:63], s17 offen offset:1024 sc1\n\t"
        ".Lsk11_%=:\n\t"
        "s_mov_b64 exec, -1\n\t"
        "s_branch .Lnext1_%=\n\t"
        ".Lready1_%=:\n\t"
        "v_lshlrev_b32 v88, 16, v80\n\t"
        "v_and_b32 v89, 0xffff0000, v80\n\t"
        "v_lshlrev_b32 v90, 16, v84\n\t"
        "v_and_b32 v91, 0xffff0000, v84\n\t"
        "v_lshlrev_b32 v92, 16, v82\n\t"
        "v_and_b32 v93, 0xffff0000, v82\n\t"
        "v_lshlrev_b32 v94, 16, v86\n\t"
        "v_and_b32 v95, 0xffff0000, v86\n\t"
        "v_pk_mul_f32 v[88:89], v[88:89], v[90:91]\n\t"
        "v_pk_mul_f32 v[92:93], v[92:93], v[94:95]\n\t"
        "s_bitset1_b32 s54, 1\n\t"
        "v_cvt_pk_bf16_f32 v80, v88, v89\n\t"
        "v_cvt_pk_bf16_f32 v81, v92, v93\n\t"
        "ds_write_b64 v12, v[80:81] offset:2048\n\t"
        ".Lnext1_%=:\n\t"
        "s_cmp_eq_u32 s54, 3\n\t"
        "s_cbranch_scc1 .Lgoth_%=\n\t"
        "s_sub_u32 s45, s45, 1\n\t"
        "s_cbranch_scc0 .Lpoll1_%=\n\t"
        "s_branch .Lcend_%=\n\t"
        ".Lgoth_%=:\n\t"
        "s_mov_b32 s48, 0\n\t"
        "s_mov_b32 s49, -1\n\t"
        ".Lgoth2_%=:\n\t"
        "s_waitcnt lgkmcnt(0)\n\t"
        "s_barrier\n\t"
        "ds_read_b128 v[72:75], v13 offset:0\n\t"
        "ds_read_b128 v[76:79], v13 offset:512\n\t"
        "ds_read_b128 v[80:83], v13 offset:1024\n\t"
        "ds_read_b128 v[84:87], v13 offset:1536\n\t"
        "ds_read_b128 v[88:91], v13 offset:2048\n\t"
        "ds_read_b128 v[92:95], v13 offset:2560\n\t"
        "ds_read_b64_tr_b16 v[64:65], v10 offset:24576\n\t"
        "ds_read_b64_tr_b16 v[66:67], v10 offset:25600\n\t"
        "ds_read_b64_tr_b16 v[68:69], v11 offset:24576\n\t"
        "ds_read_b64_tr_b16 v[70:71], v11 offset:25600\n\t"
        "ds_read_b128 v[96:99], v13 offset:3072\n\t"
        "s_waitcnt lgkmcnt(10)\n\t"
        "v_mfma_f32_16x16x32_bf16 v[104:107], v[72:75], v[16:19], 0\n\t"
        "v_mfma_f32_16x16x32_bf16 v[108:111], v[72:75], v[20:23], 0\n\t"
        "s_waitcnt lgkmcnt(9)\n\t"
        "v_mfma_f32_16x16x32_bf16 v[104:107], v[76:79], v[24:27], v[104:107]\n\t"
        "v_mfma_f32_16x16x32_bf16 v[108:111], v[76:79], v[28:31], v[108:111]\n\t"
        "s_waitcnt lgkmcnt(8)\n\t"
        "v_mfma_f32_16x16x32_bf16 v[104:107], v[80:83], v[32:35], v[104:107]\n\t"
        "v_mfma_f32_16x16x32_bf16 v[108:111], v[80:83], v[36:39], v[108:111]\n\t"
        "s_waitcnt lgkmcnt(7)\n\t"
        "v_mfma_f32_16x16x32_bf16 v[104:107], v[84:87], v[40:43], v[104:107]\n\t"
        "v_mfma_f32_16x16x32_bf16 v[108:111], v[84:87], v[44:47], v[108:111]\n\t"
        "s_waitcnt lgkmcnt(6)\n\t"
        "v_mfma_f32_16x16x32_bf16 v[104:107], v[88:91], v[48:51], v[104:107]\n\t"
        "v_mfma_f32_16x16x32_bf16 v[108:111], v[88:91], v[52:55], v[108:111]\n\t"
        "s_waitcnt lgkmcnt(5)\n\t"
        "v_mfma_f32_16x16x32_bf16 v[104:107], v[92:95], v[56:59], v[104:107]\n\t"
        "v_mfma_f32_16x16x32_bf16 v[108:111], v[92:95], v[60:63], v[108:111]\n\t"
        "s_waitcnt lgkmcnt(0)\n\t"
        "v_mfma_f32_16x16x32_bf16 v[104:107], v[96:99], v[64:67], v[104:107]\n\t"
        "v_mfma_f32_16x16x32_bf16 v[108:111], v[96:99], v[68:71], v[108:111]\n\t"
        "s_nop 7\n\t"
        "v_cndmask_b32 v116, v104, v108, s[48:49]\n\t"
        "v_cndmask_b32 v117, v105, v109, s[48:49]\n\t"
        "v_cndmask_b32 v118, v106, v110, s[48:49]\n\t"
        "v_cndmask_b32 v119, v107, v111, s[48:49]\n\t"
        "v_add_u32 v116, 0x80, v116\n\t"
        "v_add_u32 v117, 0x80, v117\n\t"
        "v_add_u32 v118, 0x80, v118\n\t"
        "v_add_u32 v119, 0x80, v119\n\t"
        "v_perm_b32 v112, v117, v116, v121\n\t"
        "v_perm_b32 v113, v120, v117, v122\n\t"
        "v_perm_b32 v114, v119, v118, v121\n\t"
        "v_perm_b32 v115, v120, v119, v123\n\t"
        "buffer_store_dwordx4 v[112:115], v14, s[60:63], s40 offen sc1\n\t"
        ".Lcend_%=:\n\t"
        "s_endpgm\n\t"
        ".Lsbody_%=:\n\t"
        "s_lshl_b32 s39, s16, 2\n\t"
        "s_add_u32 s39, s39, s37\n\t"
        "s_mul_i32 s39, s39, 3712\n\t"
        "s_add_u32 s39, s39, 0xffe76000\n\t"
        "v_lshlrev_b32 v6, 7, v1\n\t"
        "s_mov_b64 exec, 0x1fffffff\n\t"
        "buffer_load_dword v6, s[60:63], s39 offen sc1 lds\n\t"
        "s_mov_b64 exec, -1\n\t"
        "s_lshr_b32 s39, s8, 7\n\t"
        "s_sub_u32 s39, 3, s39\n\t"
        "s_lshl_b32 s39, s39, 1\n\t"
        "s_and_b32 s39, s39, 12\n\t"
        "s_lshl_b32 s41, s37, 4\n\t"
        "s_add_u32 s39, s39, s41\n\t"
        "s_add_u32 s39, s39, 2\n\t"
        "s_bfe_u32 s38, s39, 0x20001\n\t"
        "s_lshl_b32 s38, s38, 5\n\t"
        "s_lshr_b32 s39, s39, 3\n\t"
        "s_and_b32 s41, s36, 1\n\t"
        "s_mul_i32 s41, s41, 2\n\t"
        "s_add_u32 s39, s39, s41\n\t"
        "s_add_u32 s39, s39, 6\n\t"
        "s_and_b32 s39, s39, 7\n\t"
        "s_add_u32 s38, s38, s39\n\t"
        "s_add_u32 s38, s38, 24\n\t"
        "s_mul_i32 s38, s38, 0xe000\n\t"
        "s_add_u32 s38, s38, s43\n\t"
        "v_mul_u32_u24 v3, 0x1c00, v9\n\t"
        "v_lshl_add_u32 v3, v4, 7, v3\n\t"
        "s_mov_b32 exec_lo, 0x7f7f7f7f\n\t"
        "s_mov_b32 exec_hi, 0x7f7f7f7f\n\t"
        "buffer_load_dword v3, s[20:23], s38 offen lds\n\t"
        "s_mov_b64 exec, -1\n\t"
        "s_lshl_b32 s59, s16, 7\n\t"
        "s_add_u32 s59, s59, 0x1000\n\t"
        "s_cmp_eq_u32 s37, 3\n\t"
        "s_cselect_b32 s59, s59, 0x40000000\n\t"
        "s_mov_b64 s[28:29], s[8:9]\n\t"
        "s_mov_b64 s[30:31], s[22:23]\n\t"
        "s_xor_b32 s57, s47, 2\n\t"
        "s_and_b32 s57, s57, 6\n\t"
        "s_lshl_b32 s57, s57, 1\n\t"
        "s_bfe_u32 s41, s36, 0x20002\n\t"
        "s_add_u32 s57, s57, s41\n\t"
        "s_mul_i32 s57, s57, 224\n\t"
        "s_and_b32 s41, s36, 3\n\t"
        "s_lshl_b32 s41, s41, 2\n\t"
        "s_add_u32 s41, s41, s37\n\t"
        "s_add_u32 s57, s57, s41\n\t"
        "s_add_u32 s57, s57, 128\n\t"
        "s_lshl_b32 s57, s57, 11\n\t"
        "s_and_b32 s41, s47, 1\n\t"
        "s_lshl_b32 s41, s41, 10\n\t"
        "s_add_u32 s57, s57, s41\n\t"
        "v_lshlrev_b32 v10, 15, v9\n\t"
        "v_lshl_or_b32 v10, v4, 7, v10\n\t"
        "s_waitcnt vmcnt(0)\n\t"
        "buffer_load_dword v12, off, s[60:63], 0 offset:128 sc1\n\t"
        "v_readfirstlane_b32 s42, v2\n\t"
        "s_add_u32 s42, s42, 0x9e3779b9\n\t"
        "s_barrier\n\t"
        "v_mov_b32 v2, s42\n\t"
        "s_mov_b64 exec, 1\n\t"
        "buffer_store_dword v2, off, s[60:63], s59 sc1\n\t"
        "s_mov_b64 exec, -1\n\t"
        "s_mov_b32 exec_hi, 0xffff\n\t"
        "buffer_load_dword v10, s[28:31], s57 offen lds\n\t"
        "s_mov_b64 exec, -1\n\t"
        "s_waitcnt vmcnt(2)\n\t"
        "s_mov_b32 s45, 0x40000\n\t"
        ".Lpis_%=:\n\t"
        "v_readfirstlane_b32 s52, v12\n\t"
        "s_xor_b32 s52, s52, s42\n\t"
        "s_andn2_b32 s41, s52, 0x2000\n\t"
        "s_cbranch_scc1 .Lpiws_%=\n\t"
        "s_lshl_b32 s41, s16, 2\n\t"
        "s_add_u32 s41, s41, s37\n\t"
        "s_cmp_lg_u32 s41, 480\n\t"
        "s_cbranch_scc1 .Lsparewait_%=\n\t"
        "v_lshlrev_b32 v3, 7, v1\n\t"
        "s_mov_b32 s45, 0x40000\n\t"
        ".Lclose_%=:\n\t"
        "s_sleep 4\n\t"
        "s_mov_b32 s41, 0x1000\n\t"
        "buffer_load_dword v16, v3, s[60:63], s41 offen sc1\n\t"
        "s_mov_b32 s41, 0x3000\n\t"
        "buffer_load_dword v17, v3, s[60:63], s41 offen sc1\n\t"
        "s_mov_b32 s41, 0x5000\n\t"
        "buffer_load_dword v18, v3, s[60:63], s41 offen sc1\n\t"
        "s_mov_b32 s41, 0x7000\n\t"
        "buffer_load_dword v19, v3, s[60:63], s41 offen sc1\n\t"
        "s_waitcnt vmcnt(0)\n\t"
        "v_bitop3_b32 v16, v16, v17, s42 bitop3:0x7e\n\t"
        "v_bitop3_b32 v18, v18, v19, s42 bitop3:0x7e\n\t"
        "v_or_b32 v16, v16, v18\n\t"
        "v_cmp_ne_u32 vcc, 0, v16\n\t"
        "s_cbranch_vccz .Lclosed_%=\n\t"
        "s_sub_u32 s45, s45, 1\n\t"
        "s_cbranch_scc0 .Lclose_%=\n\t"
        "s_endpgm\n\t"
        ".Lclosed_%=:\n\t"
        "v_mov_b32 v2, s42\n\t"
        "s_mov_b64 exec, 1\n\t"
        "buffer_store_dword v2, off, s[60:63], 0 sc1\n\t"
        "s_mov_b64 exec, -1\n\t"
        "s_branch .Lsdone_%=\n\t"
        ".Lsparewait_%=:\n\t"
        "s_sleep 16\n\t"
        ".Lsdone_%=:\n\t"
        "s_add_u32 s60, s60, s52\n\t"
        "s_addc_u32 s61, s61, 0\n\t"
        "s_branch .Lreduce_%=\n\t"
        ".Lpiwp_%=:\n\t"
        "buffer_load_dword v120, off, s[60:63], 0 offset:128 sc1\n\t"
        "s_sub_u32 s45, s45, 1\n\t"
        "s_waitcnt vmcnt(0)\n\t"
        "s_cbranch_scc0 .Lpip_%=\n\t"
        "s_endpgm\n\t"
        ".Lpiwc_%=:\n\t"
        "buffer_load_dword v15, off, s[60:63], 0 offset:128 sc1\n\t"
        "s_sub_u32 s45, s45, 1\n\t"
        "s_waitcnt vmcnt(0)\n\t"
        "s_cbranch_scc0 .Lpic_%=\n\t"
        "s_endpgm\n\t"
        ".Lpiws_%=:\n\t"
        "buffer_load_dword v12, off, s[60:63], 0 offset:128 sc1\n\t"
        "s_sub_u32 s45, s45, 1\n\t"
        "s_waitcnt vmcnt(0)\n\t"
        "s_cbranch_scc0 .Lpis_%=\n\t"
        "s_endpgm\n\t"
        ".Lstop_%=:\n\t"
        :: "s"(__builtin_amdgcn_kernarg_segment_ptr()), "s"(blockIdx.x), "v"(threadIdx.x) : "v1", "v2", "v3", "v4", "v5", "v6", "v7", "v8", "v9", "v10", "v11", "v12", "v13", "v14", "v15", "v16", "v17", "v18", "v19", "v20", "v21", "v22", "v23", "v24", "v25", "v26", "v27", "v28", "v29", "v30", "v31", "v32", "v33", "v34", "v35", "v36", "v37", "v38", "v39", "v40", "v41", "v42", "v43", "v44", "v45", "v46", "v47", "v48", "v49", "v50", "v51", "v52", "v53", "v54", "v55", "v56", "v57", "v58", "v59", "v60", "v61", "v62", "v63", "v64", "v65", "v66", "v67", "v68", "v69", "v70", "v71", "v72", "v73", "v74", "v75", "v76", "v77", "v78", "v79", "v80", "v81", "v82", "v83", "v84", "v85", "v86", "v87", "v88", "v89", "v90", "v91", "v92", "v93", "v94", "v95", "v96", "v97", "v98", "v99", "v100", "v101", "v102", "v103", "v104", "v105", "v106", "v107", "v108", "v109", "v110", "v111", "v112", "v113", "v114", "v115", "v116", "v117", "v118", "v119", "v120", "v121", "v122", "v123", "v124", "v125", "v126", "v127", "s3", "s4", "s5", "s6", "s7", "s8", "s9", "s10", "s11", "s12", "s13", "s14", "s15", "s16", "s17", "s18", "s19", "s20", "s21", "s22", "s23", "s24", "s25", "s26", "s27", "s28", "s29", "s30", "s31", "s36", "s37", "s38", "s39", "s40", "s41", "s42", "s43", "s44", "s45", "s46", "s47", "s48", "s49", "s50", "s51", "s52", "s53", "s54", "s55", "s56", "s57", "s58", "s59", "s60", "s61", "s62", "s63", "s64", "s65", "s66", "s67", "s68", "s69", "s70", "s71", "s72", "s73", "s74", "s75", "s76", "s77", "s78", "s79", "s80", "s81", "s82", "s83", "s84", "s85", "s86", "s87", "s88", "s89", "s90", "s91", "s92", "s93", "s94", "s95", "s96", "s97", "s98", "s99", "vcc", "scc", "memory");
}

extern "C" void kernel_launch(void* const* d_in, const int* in_sizes, int n_in, void* d_out, int out_size, void* d_ws, size_t ws_size, hipStream_t stream) {
    (void)hipFuncSetAttribute((const void*)swiglu_fused, hipFuncAttributeMaxDynamicSharedMemorySize, L_TOTAL);
    swiglu_fused<<<NWG, 256, L_TOTAL, stream>>>(d_in[0], d_in[1], d_in[2], d_in[3], d_out, d_ws);
}
```
